```python
import torch

def _softmax(x, axis=-1):
    return torch.softmax(x, dim=axis)
def _gelu(x):
    return torch.nn.functional.gelu(x, approximate="tanh")
def _silu(x):
    return torch.nn.functional.silu(x)
def _sigmoid(x):
    return torch.sigmoid(x)
def _layernorm(x, axis=-1, eps=1e-5):
    m = x.mean(dim=axis, keepdim=True)
    v = x.var(dim=axis, keepdim=True, unbiased=False)
    return (x - m) / torch.sqrt(v + eps)
def _scatter_add(tgt, idx, src, axis=0):
    return tgt.clone().index_add_(axis, idx, src)
def _segment_sum(src, seg, n):
    out = torch.zeros((n,) + src.shape[1:], dtype=src.dtype)
    return out.index_add_(0, seg, src)
def _pad(x, pw):
    flat = tuple(v for pair in reversed(pw) for v in pair)
    return torch.nn.functional.pad(x, flat)

kernel_name = 'standalone__molecular_angular_gemnet__graph'

def setup_inputs():

    _w = (lambda a: (lambda t: t.to(torch.bfloat16) if t.is_floating_point() else t)(torch.as_tensor(a)))
    N, deg, De, L = 50000, 16, 4, 2
    E = N * deg
    T = E * deg
    NR = 8
    dist = torch.rand(E).to(float)
    angle = (torch.rand(T) * torch.pi).to(float)
    centers = torch.linspace(0.0, 1.0, NR).to(float)
    ang_freq = torch.arange(1, L + 1).to(float)
    return {
        "m": _w((lambda *s: torch.randn(*s, dtype=torch.bfloat16) * 0.05)(E, De)),
        "idx_in": _w((lambda lo, hi, *s: torch.randint(lo, hi, s))(0, E, T).to(torch.int64)),
        "idx_out": _w((lambda lo, hi, *s: torch.randint(lo, hi, s))(0, E, T).to(torch.int64)),
        "dist": _w(dist),
        "angle": _w(angle),
        "centers": _w(centers),
        "ang_freq": _w(ang_freq),
        "w_rbf": _w((lambda *s: torch.randn(*s, dtype=torch.bfloat16) * 0.2)(NR, De)),
        "w_sbf": _w((lambda *s: torch.randn(*s, dtype=torch.bfloat16) * 0.2)(NR * L, De)),
        "w_down": _w((lambda *s: torch.randn(*s, dtype=torch.bfloat16) * 0.2)(De, De)),
        "w_up": _w((lambda *s: torch.randn(*s, dtype=torch.bfloat16) * 0.2)(De, De)),
    }

def reference(m, idx_in, idx_out, dist, angle, centers, ang_freq, w_rbf, w_sbf, w_down, w_up):
    E, De = m.shape[0], m.shape[1]
    NR = centers.shape[0]
    L = ang_freq.shape[0]
    rbf = torch.exp(-10.0 * (dist[:, None] - centers[None, :]) ** 2)
    cbf = torch.cos(ang_freq[None, :] * angle[:, None])
    rbf_t = rbf[idx_out]
    sbf = torch.reshape(rbf_t[:, :, None] * cbf[:, None, :], (-1, NR * L))
    x = (m[idx_in] @ w_down) * (sbf @ w_sbf)
    agg = _segment_sum(x, idx_out, E)
    return m + (agg @ w_up) * (rbf @ w_rbf)
```

```cpp
#include <hip/hip_runtime.h>
#include <hip/hip_bf16.h>
#include <stdint.h>

__device__ __forceinline__ float bf2f(unsigned short u){ return __uint_as_float(((unsigned)u)<<16); }
__device__ __forceinline__ float rbf16(float x){
  unsigned u=__float_as_uint(x); u += 0x7FFF + ((u>>16)&1); return __uint_as_float(u & 0xFFFF0000u);
}

__global__ void zero_k(float4* p, int n){ int i=blockIdx.x*blockDim.x+threadIdx.x; if(i<n) p[i]=make_float4(0,0,0,0);}

__global__ void trip_k(const unsigned short* m,const int* idx_in,const int* idx_out,const unsigned short* dist,
  const unsigned short* angle,const unsigned short* centers,const unsigned short* freq,const unsigned short* w_sbf,
  const unsigned short* w_down, float* agg,int T){
  int t=blockIdx.x*blockDim.x+threadIdx.x; if(t>=T) return;
  int ei=idx_in[t], eo=idx_out[t];
  float a=bf2f(angle[t]);
  float c[2]; for(int l=0;l<2;l++) c[l]=cosf(rbf16(bf2f(freq[l])*a));
  float d=bf2f(dist[eo]);
  float s[4]={0,0,0,0};
  for(int r=0;r<8;r++){ float x=rbf16(d-bf2f(centers[r])); x=rbf16(x*x); x=rbf16(-10.f*x); float rb=rbf16(expf(x));
    for(int l=0;l<2;l++) for(int k=0;k<4;k++) s[k]+=rb*c[l]*bf2f(w_sbf[(r*2+l)*4+k]); }
  float mm[4]; for(int k=0;k<4;k++) mm[k]=bf2f(m[(size_t)ei*4+k]);
  for(int k=0;k<4;k++){ float md=0; for(int j=0;j<4;j++) md+=mm[j]*bf2f(w_down[j*4+k]); atomicAdd(&agg[(size_t)eo*4+k], md*s[k]); }
}
__global__ void fin_k(const unsigned short* m,const unsigned short* dist,const unsigned short* centers,const unsigned short* w_rbf,const unsigned short* w_up,const float* agg,__hip_bfloat16* out,int E){
  int e=blockIdx.x*blockDim.x+threadIdx.x; if(e>=E) return;
  float d=bf2f(dist[e]); float g[4]={0,0,0,0};
  for(int r=0;r<8;r++){ float x=rbf16(d-bf2f(centers[r])); x=rbf16(x*x); x=rbf16(-10.f*x); float rb=rbf16(expf(x));
    for(int k=0;k<4;k++) g[k]+=rb*bf2f(w_rbf[r*4+k]); }
  for(int k=0;k<4;k++){ float u=0; for(int j=0;j<4;j++) u+=agg[(size_t)e*4+j]*bf2f(w_up[j*4+k]);
    out[(size_t)e*4+k]=__float2bfloat16(bf2f(m[(size_t)e*4+k])+u*g[k]); }
}

extern "C" void kernel_launch(void* const* d_in, const int* in_sizes, int n_in,
                              void* d_out, int out_size, void* d_ws, size_t ws_size,
                              hipStream_t stream) {
  const unsigned short* m=(const unsigned short*)d_in[0];
  const int* ii=(const int*)d_in[1]; const int* io=(const int*)d_in[2];
  const unsigned short* dist=(const unsigned short*)d_in[3];
  const unsigned short* ang=(const unsigned short*)d_in[4];
  const unsigned short* cen=(const unsigned short*)d_in[5];
  const unsigned short* fr=(const unsigned short*)d_in[6];
  const unsigned short* w_rbf=(const unsigned short*)d_in[7];
  const unsigned short* w_sbf=(const unsigned short*)d_in[8];
  const unsigned short* w_down=(const unsigned short*)d_in[9];
  const unsigned short* w_up=(const unsigned short*)d_in[10];
  int E=in_sizes[3], T=in_sizes[1];
  float* agg=(float*)d_ws;
  zero_k<<<(E+255)/256,256,0,stream>>>((float4*)agg,E);
  trip_k<<<(T+255)/256,256,0,stream>>>(m,ii,io,dist,ang,cen,fr,w_sbf,w_down,agg,T);
  fin_k<<<(E+255)/256,256,0,stream>>>(m,dist,cen,w_rbf,w_up,agg,(__hip_bfloat16*)d_out,E);
}
```

```cpp
#include <hip/hip_runtime.h>
#include <hip/hip_bf16.h>
#include <stdint.h>

typedef unsigned int u32;
typedef unsigned long long u64;
typedef unsigned short u16;
typedef u32 u32x2 __attribute__((ext_vector_type(2)));
typedef u32 u32x3 __attribute__((ext_vector_type(3)));
typedef u32 u32x4 __attribute__((ext_vector_type(4)));

#define FA_ROWS 300000
#define ST_NT 0

constexpr int E_ = 800000;
constexpr int T_ = 12800000;
constexpr int NB = 256;
constexpr int EB = 3125;
constexpr int NWG = 256;
constexpr int NTH = 1024;
constexpr int NT = 4;
constexpr int TILE = 12500;
constexpr int V4 = TILE / 4;
constexpr int NTILES = NWG * NT;
constexpr int TSTRIDE = 12512;
constexpr u32 FA = FA_ROWS;
constexpr u32 NULLREC = (u32)EB << 20;

constexpr size_t WS_TAB = 0;
constexpr size_t WS_MAX = WS_TAB + (size_t)NTILES * 512 * 4;
constexpr size_t WS_REC = WS_MAX + 4096;
static_assert(WS_REC % 128 == 0, "align");
static_assert(WS_REC + (size_t)NTILES * TSTRIDE * 12 <= (size_t)256 * 1024 * 1024, "ws");

__device__ __forceinline__ float bf2f(u32 h) { return __uint_as_float(h << 16); }
__device__ __forceinline__ float bflo(u32 w) { return __uint_as_float(w << 16); }
__device__ __forceinline__ float bfhi(u32 w) { return __uint_as_float(w & 0xffff0000u); }
typedef __bf16 bf16x2_t __attribute__((ext_vector_type(2)));
typedef float f32x2_t __attribute__((ext_vector_type(2)));
__device__ __forceinline__ u32 pack_bf16(float a, float b) {
  f32x2_t v = {a, b};
  bf16x2_t r = __builtin_convertvector(v, bf16x2_t);
  return __builtin_bit_cast(u32, r);
}
__device__ __forceinline__ float rbf16(float a) { return bflo(pack_bf16(a, a)); }

__device__ __forceinline__ void lds_barrier() {
  asm volatile("s_waitcnt lgkmcnt(0)\n\ts_barrier" ::: "memory");
}

template <int CTRL, int RM>
__device__ __forceinline__ u32 dpp0(u32 v) { return (u32)__builtin_amdgcn_update_dpp(0, (int)v, CTRL, RM, 0xf, false); }
__device__ __forceinline__ u32 wave_incl_add(u32 v) {
  v += dpp0<0x111, 0xf>(v);
  v += dpp0<0x112, 0xf>(v);
  v += dpp0<0x114, 0xf>(v);
  v += dpp0<0x118, 0xf>(v);
  v += dpp0<0x142, 0xa>(v);
  v += dpp0<0x143, 0xc>(v);
  return v;
}
__device__ __forceinline__ u32 umax(u32 a, u32 b) { return a > b ? a : b; }
__device__ __forceinline__ u32 wave_incl_max(u32 v) {
  v = umax(v, dpp0<0x111, 0xf>(v));
  v = umax(v, dpp0<0x112, 0xf>(v));
  v = umax(v, dpp0<0x114, 0xf>(v));
  v = umax(v, dpp0<0x118, 0xf>(v));
  v = umax(v, dpp0<0x142, 0xa>(v));
  v = umax(v, dpp0<0x143, 0xc>(v));
  return v;
}

typedef __amdgpu_buffer_rsrc_t rsrc_t;
__device__ __forceinline__ rsrc_t make_rsrc(const void* p, u32 bytes) {
  return __builtin_amdgcn_make_buffer_rsrc((void*)p, 0, bytes, 0x00020000);
}
constexpr u32 OOB = 0x80000000u;
constexpr int AUX_NT = 2;
constexpr int AUX_ST = ST_NT ? 2 : 0;

struct Raw { u32x4 in, out; u32x2 an; };

constexpr int NA_SL = (int)(((long long)TILE * FA / E_ + 400 + NTH - 1) / NTH);
constexpr int NB_SL = (int)((((long long)TILE * (E_ - FA) / E_ + 400) / 2 + 128 + NTH - 1) / NTH);

__global__ void __launch_bounds__(NTH) k_part(const u32* __restrict__ idx_in, const u32* __restrict__ idx_out,
                                              const u16* __restrict__ angle, const u32x2* __restrict__ mrow,
                                              char* __restrict__ ws) {
  __shared__ __attribute__((aligned(16))) u64 stage[TILE + 256 + 4];
  __shared__ __attribute__((aligned(16))) u32 hist[512];
  __shared__ __attribute__((aligned(16))) u32 cursor[512];
  __shared__ u32 sinfo[4];
  __shared__ u32 smax[16];

  const int t = threadIdx.x;
  const int lane = t & 63;
  const int wave = __builtin_amdgcn_readfirstlane(t >> 6);
  const int w = blockIdx.x;
  const rsrc_t r_in = make_rsrc(idx_in, T_ * 4u), r_out = make_rsrc(idx_out, T_ * 4u), r_an = make_rsrc(angle, T_ * 2u);
  const rsrc_t r_m = make_rsrc(mrow, E_ * 8u);

  Raw raw[3];
  u32 xin, xout, xan;
  constexpr int NX = TILE - 12 * NTH;
  const u32 vo16 = t * 16u, vo8 = t * 8u;
  const u32 xo4 = t < NX ? t * 4u : OOB, xo2 = t < NX ? t * 2u : OOB;
  auto load_tile = [&](int k) {
    const u32 r0 = (u32)(w * NT + k) * TILE;
    __builtin_amdgcn_sched_barrier(0);
#pragma unroll
    for (int s = 0; s < 3; s++) {
      const u32 r = r0 + s * NTH * 4;
      raw[s].in = __builtin_amdgcn_raw_buffer_load_b128(r_in, vo16, r * 4u, AUX_NT);
      raw[s].out = __builtin_amdgcn_raw_buffer_load_b128(r_out, vo16, r * 4u, AUX_NT);
      raw[s].an = __builtin_amdgcn_raw_buffer_load_b64(r_an, vo8, r * 2u, AUX_NT);
    }
    const u32 r = r0 + 12 * NTH;
    xin = __builtin_amdgcn_raw_buffer_load_b32(r_in, xo4, r * 4u, AUX_NT);
    xout = __builtin_amdgcn_raw_buffer_load_b32(r_out, xo4, r * 4u, AUX_NT);
    xan = __builtin_amdgcn_raw_buffer_load_b16(r_an, xo2, r * 2u, AUX_NT);
    __builtin_amdgcn_sched_barrier(0);
  };
  u32 lo[13], hi[13];
  auto key = [&](u32 in, u32 out, u32 an16, u32& l, u32& h) {
    u32 q;
    asm("v_mul_hi_u32_u24 %0, %1, %2" : "=v"(q) : "v"(out), "v"(1374390u));
    const u32 loc = out - q * EB;
    const u32 b4 = q * 4 + (in >= FA ? 1024u : 0u);
    l = in | (loc << 20);
    h = an16 | (b4 << 16);
    return b4;
  };
  auto keys = [&]() {
#pragma unroll
    for (int s = 0; s < 3; s++) {
#pragma unroll
      for (int j = 0; j < 4; j++) {
        const u32 aw = raw[s].an[j >> 1];
        const u32 b4 = key(raw[s].in[j], raw[s].out[j], (j & 1) ? (aw >> 16) : (aw & 0xffffu), lo[s * 4 + j], hi[s * 4 + j]);
        atomicAdd((u32*)((char*)hist + b4), 1u);
      }
    }
    const u32 b4 = key(xin, xout, xan, lo[12], hi[12]);
    if (t < NX) atomicAdd((u32*)((char*)hist + b4), 1u);
  };

  {
    u32x2 r[4];
#pragma unroll
    for (int i = 0; i < 4; i++) {
      const u32 e = i * NTH + t;
      r[i] = __builtin_amdgcn_raw_buffer_load_b64(r_m, e < EB ? e * 8u : OOB, w * EB * 8u, 0);
    }
    load_tile(0);
    u32 mx = 0;
#pragma unroll
    for (int i = 0; i < 4; i++)
      mx = umax(mx, umax(umax(r[i].x & 0x7fffu, (r[i].x >> 16) & 0x7fffu), umax(r[i].y & 0x7fffu, (r[i].y >> 16) & 0x7fffu)));
    mx = wave_incl_max(mx);
    if (lane == 63) smax[wave] = mx;
  }
  if (t < 512) hist[t] = 0;
  lds_barrier();
  if (t == 0) {
    u32 mx = 0;
#pragma unroll
    for (int i = 0; i < 16; i++) mx = umax(mx, smax[i]);
    ((u32*)(ws + WS_MAX))[w] = mx;
  }
  keys();
  load_tile(1);

#pragma unroll
  for (int k = 0; k < NT; k++) {
    const int tile = w * NT + k;
    lds_barrier();
    if (wave == 1) {
      u32x4 c0 = *(const u32x4*)&hist[lane * 8], c1 = *(const u32x4*)&hist[lane * 8 + 4];
      *(u32x4*)&hist[lane * 8] = (u32x4){0, 0, 0, 0};
      *(u32x4*)&hist[lane * 8 + 4] = (u32x4){0, 0, 0, 0};
      const bool isB = lane >= 32;
      const u32 padm = isB ? 1u : 0u;
      u32 tot = 0;
#pragma unroll
      for (int j = 0; j < 4; j++) tot += ((c0[j] + padm) & ~padm) + ((c1[j] + padm) & ~padm);
      const u32 incl = wave_incl_add(tot);
      const u32 nA = __builtin_amdgcn_readlane(incl, 31);
      const u32 all = __builtin_amdgcn_readlane(incl, 63);
      const u32 nAp = (nA + 1) & ~1u;
      u32 run = incl - tot - (isB ? nA : 0u);
      const u32 slot0 = isB ? nAp : 0u, unit0 = isB ? nA : 0u;
      const rsrc_t r_tab = make_rsrc(ws + WS_TAB + (size_t)tile * 2048, 2048);
#pragma unroll
      for (int h = 0; h < 2; h++) {
        const u32x4 c = h ? c1 : c0;
        u32x4 cur, ent;
#pragma unroll
        for (int j = 0; j < 4; j++) {
          const u32 p = (c[j] + padm) & ~padm;
          cur[j] = (slot0 + run) * 8;
          ent[j] = (unit0 + (run >> padm)) | ((p >> padm) << 16);
          stage[(isB && p) ? slot0 + run + p - 1 : (u32)(TILE + 258)] = (u64)NULLREC;
          run += p;
        }
        *(u32x4*)&cursor[lane * 8 + h * 4] = cur;
        __builtin_amdgcn_raw_buffer_store_b128(ent, r_tab, lane * 32u + h * 16u, 0, 0);
        __builtin_amdgcn_sched_barrier(0);
      }
      if (lane == 0) { sinfo[0] = nA; sinfo[1] = nAp; sinfo[2] = (all - nA) >> 1; }
    }
    lds_barrier();
    {
      u32 pos[13];
#pragma unroll
      for (int j = 0; j < 12; j++) pos[j] = atomicAdd((u32*)((char*)cursor + (hi[j] >> 16)), 8u);
      if (t < NX) pos[12] = atomicAdd((u32*)((char*)cursor + (hi[12] >> 16)), 8u);
#pragma unroll
      for (int j = 0; j < 12; j++) *(u64*)((char*)stage + pos[j]) = (u64)lo[j] | ((u64)hi[j] << 32);
      if (t < NX) *(u64*)((char*)stage + pos[12]) = (u64)lo[12] | ((u64)hi[12] << 32);
    }
    __builtin_amdgcn_sched_barrier(0);
    if (k + 1 < NT) keys();
    __builtin_amdgcn_sched_barrier(0);
    lds_barrier();
    {
      const u32 nA = __builtin_amdgcn_readfirstlane(sinfo[0]), nAp = __builtin_amdgcn_readfirstlane(sinfo[1]);
      const u32 nBu = __builtin_amdgcn_readfirstlane(sinfo[2]);
      const rsrc_t r_rec = make_rsrc(ws + WS_REC + (size_t)tile * (TSTRIDE * 12), TSTRIDE * 12);
      u32 a0 = 0, b0 = 0;
      do {
        u32x2 r[NA_SL > 0 ? NA_SL : 1], g[NA_SL > 0 ? NA_SL : 1];
#pragma unroll
        for (int u = 0; u < NA_SL; u++) {
          const u32 j = a0 + u * NTH + t;
          r[u] = *(const u32x2*)&stage[j < nA ? j : 0];
          g[u] = __builtin_amdgcn_raw_buffer_load_b64(r_m, j < nA ? (r[u].x & 0xfffffu) * 8u : OOB, 0, 0);
        }
#pragma unroll
        for (int u = 0; u < NB_SL; u++) {
          const u32 j = b0 + u * NTH + t;
          const u32x4 q = *(const u32x4*)&stage[j < nBu ? nAp + 2 * j : 0];
          const u32x3 v = {q.x, q.z, (q.y & 0xffffu) | (q.w << 16)};
          __builtin_amdgcn_raw_buffer_store_b96(v, r_rec, j < nBu ? (nA + j) * 12u : OOB, 0, AUX_ST);
        }
#pragma unroll
        for (int u = 0; u < NA_SL; u++) {
          const u32 j = a0 + u * NTH + t;
          const u32x3 v = {g[u].x, g[u].y, __builtin_amdgcn_alignbit(r[u].y, r[u].x, 20)};
          __builtin_amdgcn_raw_buffer_store_b96(v, r_rec, j < nA ? j * 12u : OOB, 0, AUX_ST);
        }
        a0 += NA_SL * NTH;
        b0 += NB_SL * NTH;
      } while ((NA_SL > 0 && a0 < nA) || b0 < nBu);
    }
    if (k + 2 < NT) load_tile(k + 2);
  }
}

constexpr float MAGIC = 12582912.0f;
constexpr u32 MAGIC_BITS = 0x4B400000u;
constexpr int EP = EB + 3;

struct AccLds {
  u64 a01[EP];
  u64 a23[EP];
  u32x4 at[EP];
  u32 cnt[EP];
  u32 kexp[EP];
  float red[16];
  u32 flag;
};

struct Consts { float wd[16]; float f0, f1; };

template <int MODE>
__device__ __forceinline__ void add_record(AccLds& S, const Consts& C, u32 m01, u32 m23, u32 loc, float ang) {
  const u32 pk = pack_bf16(C.f0 * ang, C.f1 * ang);
  const float INV2PI = 0.15915494309189535f;
  const float c0 = __builtin_amdgcn_cosf(__builtin_amdgcn_fractf(bflo(pk) * INV2PI));
  const float c1 = __builtin_amdgcn_cosf(__builtin_amdgcn_fractf(bfhi(pk) * INV2PI));
  const u32x4 a = S.at[loc];
  const float s0 = c0 * bflo(a.x) + c1 * bflo(a.z);
  const float s1 = c0 * bfhi(a.x) + c1 * bfhi(a.z);
  const float s2 = c0 * bflo(a.y) + c1 * bflo(a.w);
  const float s3 = c0 * bfhi(a.y) + c1 * bfhi(a.w);
  const float x0 = bflo(m01), x1 = bfhi(m01), x2 = bflo(m23), x3 = bfhi(m23);
  const float d0 = x0 * C.wd[0] + x1 * C.wd[4] + x2 * C.wd[8] + x3 * C.wd[12];
  const float d1 = x0 * C.wd[1] + x1 * C.wd[5] + x2 * C.wd[9] + x3 * C.wd[13];
  const float d2 = x0 * C.wd[2] + x1 * C.wd[6] + x2 * C.wd[10] + x3 * C.wd[14];
  const float d3 = x0 * C.wd[3] + x1 * C.wd[7] + x2 * C.wd[11] + x3 * C.wd[15];
  if (MODE == 0) {
    const u32 t0 = __float_as_uint(__builtin_fmaf(d0, s0, MAGIC)), t1 = __float_as_uint(__builtin_fmaf(d1, s1, MAGIC));
    const u32 t2 = __float_as_uint(__builtin_fmaf(d2, s2, MAGIC)), t3 = __float_as_uint(__builtin_fmaf(d3, s3, MAGIC));
    atomicAdd(&S.a01[loc], (u64)t0 | ((u64)t1 << 32));
    atomicAdd(&S.a23[loc], (u64)t2 | ((u64)t3 << 32));
    atomicAdd(&S.cnt[loc], 1u);
  } else {
    float* p01 = (float*)&S.a01[loc];
    float* p23 = (float*)&S.a23[loc];
    atomicAdd(p01, d0 * s0);
    atomicAdd(p01 + 1, d1 * s1);
    atomicAdd(p23, d2 * s2);
    atomicAdd(p23 + 1, d3 * s3);
  }
}

struct Runs { u32 P, cnt, base, total; };
__device__ __forceinline__ Runs make_runs(u32 ent, u32 tile) {
  Runs R;
  R.cnt = ent >> 16;
  const u32 incl = wave_incl_add(R.cnt);
  R.P = incl - R.cnt;
  R.total = __builtin_amdgcn_readlane(incl, 63);
  R.base = tile * TSTRIDE + (ent & 0xffffu) - R.P;
  return R;
}
__device__ __forceinline__ u32 window_unit(const Runs& R, u32 u0, int lane) {
  const u32 d = R.P - u0;
  const bool part = (R.cnt != 0) && (d - 1u < 63u);
  u32 v = (u32)__builtin_amdgcn_ds_permute((int)(part ? d * 4 : 0u), (int)(part ? R.base : 0u));
  const u64 m0 = __ballot(R.P <= u0);
  const int r0 = 63 - __builtin_clzll(m0);
  const u32 b0 = __builtin_amdgcn_readlane(R.base, r0);
  v = lane == 0 ? b0 : v;
  v = wave_incl_max(v);
  return v + u0 + lane;
}

__device__ __forceinline__ u32x3 load_unit(const Runs& R, rsrc_t r_rec, u32 u0, int lane) {
  const u32 u = window_unit(R, u0, lane);
  return __builtin_amdgcn_raw_buffer_load_b96(r_rec, u0 + lane < R.total ? ((u << 1) + u) << 2 : OOB, 0, 0);
}

template <int MODE>
__device__ __forceinline__ void proc_A(AccLds& S, const Consts& C, const u32x3& c, u32 u0, u32 total, int lane) {
  asm volatile("" :: "v"(c.x), "v"(c.y), "v"(c.z));
  if (u0 + lane < total) add_record<MODE>(S, C, c.x, c.y, c.z & 0xfffu, __uint_as_float((c.z << 4) & 0xffff0000u));
}

template <int MODE>
__device__ __forceinline__ void run_A(AccLds& S, const Consts& C, const Runs& R, rsrc_t r_rec, int lane) {
  const u32 total = R.total;
  if (total == 0) return;
  u32x3 x0 = load_unit(R, r_rec, 0, lane), x1 = load_unit(R, r_rec, 64, lane);
  u32 u0 = 0;
  do {
    const u32x3 y0 = load_unit(R, r_rec, u0 + 128, lane), y1 = load_unit(R, r_rec, u0 + 192, lane);
    proc_A<MODE>(S, C, x0, u0, total, lane);
    proc_A<MODE>(S, C, x1, u0 + 64, total, lane);
    x0 = load_unit(R, r_rec, u0 + 256, lane); x1 = load_unit(R, r_rec, u0 + 320, lane);
    proc_A<MODE>(S, C, y0, u0 + 128, total, lane);
    proc_A<MODE>(S, C, y1, u0 + 192, total, lane);
    u0 += 256;
  } while (u0 < total);
  __builtin_amdgcn_s_waitcnt(0x0F70);
}

struct BWin { u32 l0, l1, an; u32x2 g0, g1; };
__device__ __forceinline__ void issue_B(BWin& W, const u32x3& c, rsrc_t r_m, u32 u0, u32 total, int lane) {
  const bool v = u0 + lane < total;
  W.g0 = __builtin_amdgcn_raw_buffer_load_b64(r_m, v ? (c.x & 0xfffffu) * 8u : OOB, 0, 0);
  W.g1 = __builtin_amdgcn_raw_buffer_load_b64(r_m, v ? (c.y & 0xfffffu) * 8u : OOB, 0, 0);
  W.l0 = c.x >> 20; W.l1 = c.y >> 20; W.an = c.z;
}
template <int MODE>
__device__ __forceinline__ void proc_B(AccLds& S, const Consts& C, const BWin& W, u32 u0, u32 total, int lane) {
  asm volatile("" :: "v"(W.g0.x), "v"(W.g0.y), "v"(W.g1.x), "v"(W.g1.y));
  if (u0 + lane < total) {
    add_record<MODE>(S, C, W.g0.x, W.g0.y, W.l0, bflo(W.an));
    add_record<MODE>(S, C, W.g1.x, W.g1.y, W.l1, bfhi(W.an));
  }
}

template <int MODE>
__device__ __forceinline__ void run_B(AccLds& S, const Consts& C, const Runs& R, rsrc_t r_rec, rsrc_t r_m, int lane) {
  const u32 total = R.total;
  if (total == 0) return;
  BWin X, Y;
  u32x3 c = load_unit(R, r_rec, 0, lane);
  issue_B(X, c, r_m, 0, total, lane);
  c = load_unit(R, r_rec, 64, lane);
  u32 u0 = 0;
  do {
    issue_B(Y, c, r_m, u0 + 64, total, lane);
    c = load_unit(R, r_rec, u0 + 128, lane);
    proc_B<MODE>(S, C, X, u0, total, lane);
    issue_B(X, c, r_m, u0 + 128, total, lane);
    c = load_unit(R, r_rec, u0 + 192, lane);
    proc_B<MODE>(S, C, Y, u0 + 64, total, lane);
    u0 += 128;
  } while (u0 < total);
  __builtin_amdgcn_s_waitcnt(0x0F70);
}

__device__ __forceinline__ float wel(const u32* __restrict__ w, int i) { return (i & 1) ? bfhi(w[i >> 1]) : bflo(w[i >> 1]); }

__device__ __forceinline__ void edge_rbf(float d, const u32* __restrict__ centers, float* rb) {
#pragma unroll
  for (int r = 0; r < 8; r++) {
    float x = rbf16(d - wel(centers, r));
    x = rbf16(x * x);
    x = rbf16(-10.0f * x);
    rb[r] = rbf16(__expf(x));
  }
}

__global__ void __launch_bounds__(NTH) k_acc(const u32x2* __restrict__ mrow, const u16* __restrict__ dist,
                                             const u32* __restrict__ centers, const u32* __restrict__ freq,
                                             const u32* __restrict__ w_rbf, const u32* __restrict__ w_sbf,
                                             const u32* __restrict__ w_down, const u32* __restrict__ w_up,
                                             const char* __restrict__ ws, u32x2* __restrict__ outp) {
  __shared__ __attribute__((aligned(16))) AccLds S;
  const int t = threadIdx.x;
  const int lane = t & 63;
  const int wave = __builtin_amdgcn_readfirstlane(t >> 6);
  const int b = (blockIdx.x & 7) * 32 + (blockIdx.x >> 3);
  const rsrc_t r_rec = make_rsrc(ws + WS_REC, (u32)NTILES * TSTRIDE * 12u);
  const rsrc_t r_m = make_rsrc(mrow, E_ * 8u);
  const rsrc_t r_dist = make_rsrc(dist + b * EB, EB * 2u);
  const rsrc_t r_mb = make_rsrc(mrow + b * EB, EB * 8u);
  const rsrc_t r_o = make_rsrc(outp + b * EB, EB * 8u);

  const u32 tile = lane * 16 + wave;
  const u32* tab = (const u32*)(ws + WS_TAB) + (size_t)tile * 512;
  const u32 entA = tab[b], entB = tab[256 + b];
  const u32 mxw = ((const u32*)(ws + WS_MAX))[t & (NWG - 1)];
  u32 dv[4];
#pragma unroll
  for (int j = 0; j < 4; j++) dv[j] = __builtin_amdgcn_raw_buffer_load_b16(r_dist, (j * NTH + t) * 2u, 0, 0);

  if (t == 0) S.flag = 0;
#pragma unroll
  for (int j = 0; j < 4; j++) {
    const int i = j * NTH + t;
    if (i < EP) {
      float rb[8];
      edge_rbf(bf2f(dv[j]), centers, rb);
      float A[8];
#pragma unroll
      for (int q = 0; q < 8; q++) A[q] = 0.f;
#pragma unroll
      for (int r = 0; r < 8; r++) {
#pragma unroll
        for (int d = 0; d < 4; d++) {
          A[d] += rb[r] * wel(w_sbf, (r * 2) * 4 + d);
          A[4 + d] += rb[r] * wel(w_sbf, (r * 2 + 1) * 4 + d);
        }
      }
      u32 ke = 0;
#pragma unroll
      for (int d = 0; d < 4; d++) {
        const float n = fabsf(A[d]) + fabsf(A[4 + d]);
        int e = __builtin_amdgcn_frexp_expf(n);
        e = e < -126 ? -126 : (e > 127 ? 127 : e);
        A[d] = __builtin_amdgcn_ldexpf(A[d], -e);
        A[4 + d] = __builtin_amdgcn_ldexpf(A[4 + d], -e);
        ke |= (u32)(e & 0xff) << (8 * d);
      }
      const bool real = i < EB;
      S.at[i] = real ? (u32x4){pack_bf16(A[0], A[1]), pack_bf16(A[2], A[3]), pack_bf16(A[4], A[5]), pack_bf16(A[6], A[7])}
                     : (u32x4){0, 0, 0, 0};
      S.kexp[i] = ke;
      S.a01[i] = 0; S.a23[i] = 0; S.cnt[i] = 0;
    }
    __builtin_amdgcn_sched_barrier(0);
  }

  const u32 mb = __builtin_amdgcn_readlane(wave_incl_max(mxw), 63);
  if (lane == 0) S.red[wave] = __uint_as_float(mb);
  lds_barrier();
  u32 mall = 0;
#pragma unroll
  for (int i = 0; i < 4; i++) mall = umax(mall, __float_as_uint(S.red[i]));
  mall = __builtin_amdgcn_readfirstlane(mall);
  const float Bm = bf2f(mall);
  Consts C;
  float invS[4];
  bool fixed_ok = true;
#pragma unroll
  for (int d = 0; d < 4; d++) {
    float cs = 0.f;
#pragma unroll
    for (int k = 0; k < 4; k++) cs += fabsf(wel(w_down, k * 4 + d));
    const float B = Bm * cs;
    const bool zero = (B == 0.f);
    if (!zero && !(B > 1e-25f && B < 1e25f)) fixed_ok = false;
    const float sc = zero ? 0.f : 4152360.0f / B;
    invS[d] = zero ? 0.f : B / 4152360.0f;
#pragma unroll
    for (int k = 0; k < 4; k++) C.wd[k * 4 + d] = __uint_as_float(__builtin_amdgcn_readfirstlane(__float_as_uint(wel(w_down, k * 4 + d) * sc)));
  }
  C.f0 = wel(freq, 0);
  C.f1 = wel(freq, 1);

  const Runs RA = make_runs(entA, tile), RB = make_runs(entB, tile);
  bool use_float = !fixed_ok;
  if (!use_float) {
#pragma unroll 1
    for (int ph = 0; ph < 2; ph++) {
      if ((ph ^ wave) & 1) run_A<0>(S, C, RA, r_rec, lane);
      else run_B<0>(S, C, RB, r_rec, r_m, lane);
    }
    lds_barrier();
    u32 over = 0;
    for (int i = t; i < EB; i += NTH) over |= (S.cnt[i] >= 512u);
    if (over) S.flag = 1;
    lds_barrier();
    use_float = __builtin_amdgcn_readfirstlane(S.flag) != 0;
    if (use_float) {
      for (int i = t; i < EP; i += NTH) { S.a01[i] = 0; S.a23[i] = 0; }
      lds_barrier();
    }
  }
  if (use_float) {
#pragma unroll
    for (int d = 0; d < 4; d++) {
      invS[d] = 1.f;
#pragma unroll
      for (int k = 0; k < 4; k++) C.wd[k * 4 + d] = wel(w_down, k * 4 + d);
    }
    run_A<1>(S, C, RA, r_rec, lane);
    run_B<1>(S, C, RB, r_rec, r_m, lane);
    lds_barrier();
  }

  u32x2 mr[4];
#pragma unroll
  for (int j = 0; j < 4; j++) mr[j] = __builtin_amdgcn_raw_buffer_load_b64(r_mb, (j * NTH + t) * 8u, 0, 0);
#pragma unroll
  for (int j = 0; j < 4; j++) {
    const int i = j * NTH + t;
    if (i < EB) {
      float ag[4];
      const u64 w01 = S.a01[i], w23 = S.a23[i];
      if (!use_float) {
        const u64 off = (u64)S.cnt[i] * (((u64)MAGIC_BITS << 32) + MAGIC_BITS);
        const u64 v01 = w01 - off, v23 = w23 - off;
        const int l0 = (int)(u32)v01, l2 = (int)(u32)v23;
        const int h1 = (int)(u32)((v01 - (u64)(long long)l0) >> 32), h3 = (int)(u32)((v23 - (u64)(long long)l2) >> 32);
        ag[0] = (float)l0; ag[1] = (float)h1; ag[2] = (float)l2; ag[3] = (float)h3;
      } else {
        ag[0] = __uint_as_float((u32)w01); ag[1] = __uint_as_float((u32)(w01 >> 32));
        ag[2] = __uint_as_float((u32)w23); ag[3] = __uint_as_float((u32)(w23 >> 32));
      }
      const u32 ke = S.kexp[i];
#pragma unroll
      for (int d = 0; d < 4; d++) ag[d] = __builtin_amdgcn_ldexpf(ag[d] * invS[d], (int)(signed char)(ke >> (8 * d)));
      float rb[8];
      edge_rbf(bf2f(dv[j]), centers, rb);
      const float mv[4] = {bflo(mr[j].x), bfhi(mr[j].x), bflo(mr[j].y), bfhi(mr[j].y)};
      float o[4];
#pragma unroll
      for (int d = 0; d < 4; d++) {
        float g = 0.f, u = 0.f;
#pragma unroll
        for (int r = 0; r < 8; r++) g += rb[r] * wel(w_rbf, r * 4 + d);
#pragma unroll
        for (int k = 0; k < 4; k++) u += ag[k] * wel(w_up, k * 4 + d);
        o[d] = mv[d] + u * g;
      }
      __builtin_amdgcn_raw_buffer_store_b64((u32x2){pack_bf16(o[0], o[1]), pack_bf16(o[2], o[3])}, r_o, i * 8u, 0, 0);
    }
    __builtin_amdgcn_sched_barrier(0);
  }
}

extern "C" void kernel_launch(void* const* d_in, const int* in_sizes, int n_in, void* d_out, int out_size,
                              void* d_ws, size_t ws_size, hipStream_t stream) {
  const u32x2* m = (const u32x2*)d_in[0];
  const u32* idx_in = (const u32*)d_in[1];
  const u32* idx_out = (const u32*)d_in[2];
  const u16* dist = (const u16*)d_in[3];
  const u16* angle = (const u16*)d_in[4];
  const u32* centers = (const u32*)d_in[5];
  const u32* freq = (const u32*)d_in[6];
  const u32* w_rbf = (const u32*)d_in[7];
  const u32* w_sbf = (const u32*)d_in[8];
  const u32* w_down = (const u32*)d_in[9];
  const u32* w_up = (const u32*)d_in[10];
  k_part<<<NWG, NTH, 0, stream>>>(idx_in, idx_out, angle, m, (char*)d_ws);
  k_acc<<<NB, NTH, 0, stream>>>(m, dist, centers, freq, w_rbf, w_sbf, w_down, w_up, (const char*)d_ws, (u32x2*)d_out);
}
```

```cpp
#include <hip/hip_runtime.h>
#include <hip/hip_bf16.h>
#include <stdint.h>

typedef unsigned int u32;
typedef unsigned long long u64;
typedef unsigned short u16;
typedef u32 u32x2 __attribute__((ext_vector_type(2)));
typedef u32 u32x3 __attribute__((ext_vector_type(3)));
typedef u32 u32x4 __attribute__((ext_vector_type(4)));
typedef float f32x4 __attribute__((ext_vector_type(4)));
typedef short s16x4 __attribute__((ext_vector_type(4)));
typedef __bf16 bf16x2_t __attribute__((ext_vector_type(2)));
typedef float f32x2_t __attribute__((ext_vector_type(2)));

#define ABL 0
#define PREF 0
#define ST_AUX 0
#define REC_AUX 0

constexpr int E_ = 800000;
constexpr int T_ = 12800000;
constexpr int NB = 256;
constexpr int EB = 3125;
constexpr int EH = E_ / 2;
constexpr int NWG = 256;
constexpr int NTH = 1024;
constexpr int NT = 4;
constexpr int TILE = 12500;
constexpr int NTILES = NWG * NT;
constexpr int NSLOT = TILE + 512;
constexpr int TSTRIDE = 6528;
constexpr u32 NULLREC = (u32)EB;

constexpr size_t WS_TAB = 0;
constexpr size_t WS_MAX = WS_TAB + (size_t)NTILES * 512 * 4;
constexpr size_t WS_REC = WS_MAX + 4096;
static_assert(WS_REC % 128 == 0, "align");
static_assert(NSLOT / 2 <= TSTRIDE, "tile region");
static_assert(WS_REC + (size_t)NTILES * TSTRIDE * 12 <= (size_t)256 * 1024 * 1024, "ws");

__device__ __forceinline__ float bf2f(u32 h) { return __uint_as_float(h << 16); }
__device__ __forceinline__ float bflo(u32 w) { return __uint_as_float(w << 16); }
__device__ __forceinline__ float bfhi(u32 w) { return __uint_as_float(w & 0xffff0000u); }
__device__ __forceinline__ u32 pack_bf16(float a, float b) {
  f32x2_t v = {a, b};
  bf16x2_t r = __builtin_convertvector(v, bf16x2_t);
  return __builtin_bit_cast(u32, r);
}

__device__ __forceinline__ void lds_barrier() {
  asm volatile("s_waitcnt lgkmcnt(0)\n\ts_barrier" ::: "memory");
}

template <int CTRL, int RM>
__device__ __forceinline__ u32 dpp0(u32 v) { return (u32)__builtin_amdgcn_update_dpp(0, (int)v, CTRL, RM, 0xf, false); }
__device__ __forceinline__ u32 wave_incl_add(u32 v) {
  v += dpp0<0x111, 0xf>(v);
  v += dpp0<0x112, 0xf>(v);
  v += dpp0<0x114, 0xf>(v);
  v += dpp0<0x118, 0xf>(v);
  v += dpp0<0x142, 0xa>(v);
  v += dpp0<0x143, 0xc>(v);
  return v;
}
__device__ __forceinline__ u32 umax(u32 a, u32 b) { return a > b ? a : b; }
__device__ __forceinline__ u32 wave_incl_max(u32 v) {
  v = umax(v, dpp0<0x111, 0xf>(v));
  v = umax(v, dpp0<0x112, 0xf>(v));
  v = umax(v, dpp0<0x114, 0xf>(v));
  v = umax(v, dpp0<0x118, 0xf>(v));
  v = umax(v, dpp0<0x142, 0xa>(v));
  v = umax(v, dpp0<0x143, 0xc>(v));
  return v;
}

typedef __amdgpu_buffer_rsrc_t rsrc_t;
__device__ __forceinline__ rsrc_t make_rsrc(const void* p, u32 bytes) {
  return __builtin_amdgcn_make_buffer_rsrc((void*)p, 0, bytes, 0x00020000);
}
constexpr u32 OOB = 0x80000000u;
constexpr int AUX_NT = 2;

#define ST(i)
#define RT(i)

struct Raw { u32x4 in, out; u32x2 an; };

__global__ void __launch_bounds__(NTH) k_part(const u32* __restrict__ idx_in, const u32* __restrict__ idx_out,
                                              const u16* __restrict__ angle, const u32x2* __restrict__ mrow,
                                              char* __restrict__ ws) {
  __shared__ __attribute__((aligned(16))) u64 stage[NSLOT + 4];
  __shared__ __attribute__((aligned(16))) u32 hist[512];
  __shared__ __attribute__((aligned(16))) u32 cursor[512];
  __shared__ u32 sinfo[4];
  __shared__ u32 smax[16];

  const int t = threadIdx.x;
  const int lane = t & 63;
  const int wave = __builtin_amdgcn_readfirstlane(t >> 6);
  const int w = blockIdx.x;
  const rsrc_t r_in = make_rsrc(idx_in, T_ * 4u), r_out = make_rsrc(idx_out, T_ * 4u), r_an = make_rsrc(angle, T_ * 2u);

  Raw raw[3];
  u32 xin, xout, xan;
  constexpr int NX = TILE - 12 * NTH;
  const u32 vo16 = t * 16u, vo8 = t * 8u;
  const u32 xo4 = t < NX ? t * 4u : OOB, xo2 = t < NX ? t * 2u : OOB;
  auto load_slot = [&](int k, int s) {
    const u32 r = (u32)(w * NT + k) * TILE + s * NTH * 4;
    __builtin_amdgcn_sched_barrier(0);
    if (s < 3) {
      raw[s].in = __builtin_amdgcn_raw_buffer_load_b128(r_in, vo16, r * 4u, AUX_NT);
      raw[s].out = __builtin_amdgcn_raw_buffer_load_b128(r_out, vo16, r * 4u, AUX_NT);
      raw[s].an = __builtin_amdgcn_raw_buffer_load_b64(r_an, vo8, r * 2u, AUX_NT);
    } else {
      xin = __builtin_amdgcn_raw_buffer_load_b32(r_in, xo4, r * 4u, AUX_NT);
      xout = __builtin_amdgcn_raw_buffer_load_b32(r_out, xo4, r * 4u, AUX_NT);
      xan = __builtin_amdgcn_raw_buffer_load_b16(r_an, xo2, r * 2u, AUX_NT);
    }
    __builtin_amdgcn_sched_barrier(0);
  };
  u32 lo[13], hi[13];
  auto key = [&](u32 in, u32 out, u32 an16, u32& l, u32& h) {
    u32 q;
    asm("v_mul_hi_u32_u24 %0, %1, %2" : "=v"(q) : "v"(out), "v"(1374390u));
    const u32 loc = out - q * EB;
    const bool up = in >= (u32)EH;
    const u32 b4 = q * 4 + (up ? 1024u : 0u);
    l = ((in - (up ? (u32)EH : 0u)) << 12) | loc;
    h = an16 | (b4 << 16);
    return b4;
  };

  ST(0); RT(48);
  {
    const rsrc_t r_m = make_rsrc(mrow + w * EB, EB * 8u);
    u32x2 r[4];
#pragma unroll
    for (int i = 0; i < 4; i++) r[i] = __builtin_amdgcn_raw_buffer_load_b64(r_m, vo8, i * NTH * 8u, 0);
#pragma unroll
    for (int s = 0; s < 4; s++) load_slot(0, s);
    u32 mx = 0;
#pragma unroll
    for (int i = 0; i < 4; i++) {
      const u32 keep = (i * NTH + t < EB) ? 0x7fffu : 0u;
      mx = umax(mx, umax(umax(r[i].x & keep, (r[i].x >> 16) & keep), umax(r[i].y & keep, (r[i].y >> 16) & keep)));
    }
    mx = wave_incl_max(mx);
    if (lane == 63) smax[wave] = mx;
  }
  if (t < 512) hist[t] = 0;
  lds_barrier();
  if (t == 0) {
    u32 mx = 0;
#pragma unroll
    for (int i = 0; i < 16; i++) mx = umax(mx, smax[i]);
    ((u32*)(ws + WS_MAX))[w] = mx;
  }
  ST(1);

#pragma unroll
  for (int k = 0; k < NT; k++) {
    const int tile = w * NT + k;
#pragma unroll
    for (int s = 0; s < 3; s++) {
#pragma unroll
      for (int j = 0; j < 4; j++) {
        const u32 aw = raw[s].an[j >> 1];
        const u32 b4 = key(raw[s].in[j], raw[s].out[j], (j & 1) ? (aw >> 16) : (aw & 0xffffu), lo[s * 4 + j], hi[s * 4 + j]);
        atomicAdd((u32*)((char*)hist + b4), 8u);
      }
    }
    {
      const u32 b4 = key(xin, xout, xan, lo[12], hi[12]);
      if (t < NX) atomicAdd((u32*)((char*)hist + b4), 8u);
    }
    if (k + 1 < NT) load_slot(k + 1, 0);
    ST(2 + k * 6);
    lds_barrier();
    ST(3 + k * 6);
    u32x4 c0, c1;
    u32 run;
    if (wave == 1) {
      c0 = *(const u32x4*)&hist[lane * 8];
      c1 = *(const u32x4*)&hist[lane * 8 + 4];
      u32x4 p0 = (c0 + 8u) & ~15u, p1 = (c1 + 8u) & ~15u;
      const u32 tot = p0.x + p0.y + p0.z + p0.w + p1.x + p1.y + p1.z + p1.w;
      const u32 incl = wave_incl_add(tot);
      run = incl - tot;
      u32x4 a, b;
      a.x = run; a.y = a.x + p0.x; a.z = a.y + p0.y; a.w = a.z + p0.z;
      b.x = a.w + p0.w; b.y = b.x + p1.x; b.z = b.y + p1.y; b.w = b.z + p1.z;
      *(u32x4*)&cursor[lane * 8] = a;
      *(u32x4*)&cursor[lane * 8 + 4] = b;
      if (lane == 63) sinfo[0] = incl >> 4;
    }
    lds_barrier();
    ST(4 + k * 6);
    if (wave == 1) {
      *(u32x4*)&hist[lane * 8] = (u32x4){0, 0, 0, 0};
      *(u32x4*)&hist[lane * 8 + 4] = (u32x4){0, 0, 0, 0};
      const rsrc_t r_tab = make_rsrc(ws + WS_TAB + (size_t)tile * 2048, 2048);
#pragma unroll
      for (int h = 0; h < 2; h++) {
        const u32x4 c = h ? c1 : c0;
        u32x4 ent;
#pragma unroll
        for (int j = 0; j < 4; j++) {
          const u32 p = (c[j] + 8u) & ~15u;
          ent[j] = (run >> 4) | (p << 12);
          *(u64*)((char*)stage + ((c[j] & 8u) ? run + c[j] : (u32)(NSLOT * 8))) = (u64)NULLREC;
          run += p;
        }
        __builtin_amdgcn_raw_buffer_store_b128(ent, r_tab, lane * 32u + h * 16u, 0, 0);
        __builtin_amdgcn_sched_barrier(0);
      }
    }
    {
      u32 pos[13];
#pragma unroll
      for (int j = 0; j < 12; j++) pos[j] = atomicAdd((u32*)((char*)cursor + (hi[j] >> 16)), 8u);
      if (t < NX) pos[12] = atomicAdd((u32*)((char*)cursor + (hi[12] >> 16)), 8u);
      if (k + 1 < NT) load_slot(k + 1, 1);
#pragma unroll
      for (int j = 0; j < 12; j++) *(u64*)((char*)stage + pos[j]) = (u64)lo[j] | ((u64)hi[j] << 32);
      if (t < NX) *(u64*)((char*)stage + pos[12]) = (u64)lo[12] | ((u64)hi[12] << 32);
    }
    if (k + 1 < NT) { load_slot(k + 1, 2); load_slot(k + 1, 3); }
    ST(5 + k * 6);
    lds_barrier();
    ST(6 + k * 6);
    {
      const u32 nu = __builtin_amdgcn_readfirstlane(sinfo[0]);
      const rsrc_t r_rec = make_rsrc(ws + WS_REC + (size_t)tile * (TSTRIDE * 12), TSTRIDE * 12);
#pragma unroll
      for (int u = 0; u < 7; u++) {
        const u32 j = u * NTH + t;
        if (u < 6 || wave < 6) {
          const u32x4 q = *(const u32x4*)&stage[j < nu ? 2 * j : 0];
          const u32x3 v = {q.x, q.z, (q.y & 0xffffu) | (q.w << 16)};
          __builtin_amdgcn_raw_buffer_store_b96(v, r_rec, j < nu ? j * 12u : OOB, 0, ST_AUX);
        }
      }
    }
    ST(7 + k * 6);
  }
}

constexpr float MAGIC = 12582912.0f;
constexpr u32 MAGIC_BITS = 0x4B400000u;
constexpr int EP = EB + 3;

struct AccLds {
  u64 a01[EP];
  u64 a23[EP];
  u32x4 at[EP];
  u32 cnt[EP];
  u32 kexp[EP];
  u32 red[16];
  u32 flag;
};

struct Consts { u32x2 wdT; float f0, f1; };

__device__ __forceinline__ float dot2bf(u32 a, u32 b) {
  return __builtin_amdgcn_fdot2_f32_bf16(__builtin_bit_cast(bf16x2_t, a), __builtin_bit_cast(bf16x2_t, b), 0.f, false);
}

template <int MODE>
__device__ __forceinline__ void add_record(AccLds& S, const Consts& C, const f32x4& md, u32 loc, float ang) {
  const u32 pk = pack_bf16(C.f0 * ang, C.f1 * ang);
  const float INV2PI = 0.15915494309189535f;
  const float c0 = __builtin_amdgcn_cosf(__builtin_amdgcn_fractf(bflo(pk) * INV2PI));
  const float c1 = __builtin_amdgcn_cosf(__builtin_amdgcn_fractf(bfhi(pk) * INV2PI));
  const u32 cp = pack_bf16(c0, c1);
  const u32x4 a = S.at[loc];
  const float s0 = dot2bf(a.x, cp), s1 = dot2bf(a.y, cp), s2 = dot2bf(a.z, cp), s3 = dot2bf(a.w, cp);
  if (MODE == 0) {
    const u32 t0 = __float_as_uint(__builtin_fmaf(md.x, s0, MAGIC)), t1 = __float_as_uint(__builtin_fmaf(md.y, s1, MAGIC));
    const u32 t2 = __float_as_uint(__builtin_fmaf(md.z, s2, MAGIC)), t3 = __float_as_uint(__builtin_fmaf(md.w, s3, MAGIC));
    atomicAdd(&S.a01[loc], (u64)t0 | ((u64)t1 << 32));
    atomicAdd(&S.a23[loc], (u64)t2 | ((u64)t3 << 32));
    atomicAdd(&S.cnt[loc], 1u);
  } else {
    float* p01 = (float*)&S.a01[loc];
    float* p23 = (float*)&S.a23[loc];
    atomicAdd(p01, md.x * s0);
    atomicAdd(p01 + 1, md.y * s1);
    atomicAdd(p23, md.z * s2);
    atomicAdd(p23 + 1, md.w * s3);
  }
}

__device__ __forceinline__ f32x4 down(const Consts& C, const u32x2& row) {
  const f32x4 z = {0.f, 0.f, 0.f, 0.f};
  return __builtin_amdgcn_mfma_f32_4x4x4bf16_1k(__builtin_bit_cast(s16x4, C.wdT), __builtin_bit_cast(s16x4, row), z, 0, 0, 0);
}

struct Runs { u32 P4, base, total; };
__device__ __forceinline__ Runs make_runs(u32 ent, u32 tile) {
  Runs R;
  const u32 cnt = ent >> 16;
  const u32 incl = wave_incl_add(cnt);
  const u32 P = incl - cnt;
  R.total = __builtin_amdgcn_readlane(incl, 63);
  R.base = tile * TSTRIDE + (ent & 0xffffu) - P;
  R.P4 = cnt ? P * 4 : 0x7ffffff0u;
  return R;
}
__device__ __forceinline__ u32x3 load_unit(const Runs& R, rsrc_t r_rec, u32 u0, int lane) {
  const u32 d4 = R.P4 - u0 * 4;
  const bool part = d4 - 4u < 252u;
  u32 v = (u32)__builtin_amdgcn_ds_permute((int)(part ? d4 : 0u), (int)R.base);
  const u64 m0 = __ballot(R.P4 <= u0 * 4);
  const int r0 = 63 - __builtin_clzll(m0 | 1ull);
  const u32 b0 = __builtin_amdgcn_readlane(R.base, r0);
  v = lane == 0 ? b0 : v;
  v = wave_incl_max(v);
  const u32 u = u0 + lane;
  return __builtin_amdgcn_raw_buffer_load_b96(r_rec, u < R.total ? __umul24(v + u, 12u) : OOB, 0, REC_AUX);
}

constexpr int NW = 14;
struct Burst { u32x3 u[NW]; };
__device__ __forceinline__ void load_burst(Burst& B, const Runs& R, rsrc_t r_rec, u32 u0, int lane) {
#pragma unroll
  for (int j = 0; j < NW; j++) B.u[j] = load_unit(R, r_rec, u0 + j * 64, lane);
}

struct Win { u32x2 g0, g1; };
__device__ __forceinline__ void issue_win(Win& W, const u32x3& c, rsrc_t r_m, u32 u0, u32 total, int lane) {
  const bool v = u0 + lane < total;
  W.g0 = __builtin_amdgcn_raw_buffer_load_b64(r_m, v ? (c.x >> 9) & ~7u : OOB, 0, 0);
  W.g1 = __builtin_amdgcn_raw_buffer_load_b64(r_m, v ? (c.y >> 9) & ~7u : OOB, 0, 0);
}
template <int MODE>
__device__ __forceinline__ void proc_win(AccLds& S, const Consts& C, const Win& W, const u32x3& c, u32 u0, u32 total, int lane) {
  const f32x4 md0 = down(C, W.g0), md1 = down(C, W.g1);
  if (u0 + lane < total) {
    add_record<MODE>(S, C, md0, c.x & 0xfffu, bflo(c.z));
    add_record<MODE>(S, C, md1, c.y & 0xfffu, bfhi(c.z));
  }
}

#define GDEPTH 2
template <int MODE>
__device__ __forceinline__ void run_burst(AccLds& S, const Consts& C, const Burst& B, u32 u0, u32 total, rsrc_t r_m, int lane) {
  Win W[GDEPTH + 1];
#pragma unroll
  for (int j = 0; j < GDEPTH; j++) issue_win(W[j], B.u[j], r_m, u0 + j * 64, total, lane);
#pragma unroll
  for (int j = 0; j < NW; j++) {
    if (j + GDEPTH < NW) issue_win(W[(j + GDEPTH) % (GDEPTH + 1)], B.u[j + GDEPTH], r_m, u0 + (j + GDEPTH) * 64, total, lane);
    proc_win<MODE>(S, C, W[j % (GDEPTH + 1)], B.u[j], u0 + j * 64, total, lane);
  }
}

template <int MODE>
__device__ __forceinline__ void run_simple(AccLds& S, const Consts& C, const Runs& R, u32 u0, rsrc_t r_rec, rsrc_t r_m, int lane) {
#pragma unroll 1
  for (; u0 < R.total; u0 += 64) {
    const u32x3 c = load_unit(R, r_rec, u0, lane);
    Win W;
    issue_win(W, c, r_m, u0, R.total, lane);
    proc_win<MODE>(S, C, W, c, u0, R.total, lane);
  }
}

__device__ __forceinline__ float wel(const u32* __restrict__ w, int i) { return (i & 1) ? bfhi(w[i >> 1]) : bflo(w[i >> 1]); }

__device__ __forceinline__ u32x4 edge_rbf(float d, const u32* __restrict__ centers) {
  u32x4 rb;
#pragma unroll
  for (int r = 0; r < 4; r++) {
    u32 p = pack_bf16(d - wel(centers, 2 * r), d - wel(centers, 2 * r + 1));
    float x = bflo(p), y = bfhi(p);
    p = pack_bf16(x * x, y * y);
    p = pack_bf16(-10.0f * bflo(p), -10.0f * bfhi(p));
    rb[r] = pack_bf16(__expf(bflo(p)), __expf(bfhi(p)));
  }
  return rb;
}
__device__ __forceinline__ f32x4 rb_times(const u32x4& rb, const u32x2& wlo, const u32x2& whi) {
  f32x4 acc = {0.f, 0.f, 0.f, 0.f};
  acc = __builtin_amdgcn_mfma_f32_4x4x4bf16_1k(__builtin_bit_cast(s16x4, wlo), __builtin_bit_cast(s16x4, ((u32x2){rb.x, rb.y})), acc, 0, 0, 0);
  acc = __builtin_amdgcn_mfma_f32_4x4x4bf16_1k(__builtin_bit_cast(s16x4, whi), __builtin_bit_cast(s16x4, ((u32x2){rb.z, rb.w})), acc, 0, 0, 0);
  return acc;
}
__device__ __forceinline__ u32x2 wcol(const u16* __restrict__ w, int r0, int rs, int c) {
  return (u32x2){(u32)w[r0 * rs + c] | ((u32)w[(r0 + 1) * rs + c] << 16), (u32)w[(r0 + 2) * rs + c] | ((u32)w[(r0 + 3) * rs + c] << 16)};
}

__global__ void __launch_bounds__(NTH) k_acc(const u32x2* __restrict__ mrow, const u16* __restrict__ dist,
                                             const u32* __restrict__ centers, const u32* __restrict__ freq,
                                             const u16* __restrict__ w_rbf, const u16* __restrict__ w_sbf,
                                             const u32* __restrict__ w_down, const u32* __restrict__ w_up,
                                             const char* __restrict__ ws, u32x2* __restrict__ outp) {
  __shared__ __attribute__((aligned(16))) AccLds S;
  const int t = threadIdx.x;
  const int lane = t & 63;
  const int wave = __builtin_amdgcn_readfirstlane(t >> 6);
  const int b = (blockIdx.x & 7) * 32 + (blockIdx.x >> 3);
  const int c4 = lane & 3;
  const rsrc_t r_rec = make_rsrc(ws + WS_REC, (u32)NTILES * TSTRIDE * 12u);
  const rsrc_t r_dist = make_rsrc(dist + b * EB, EB * 2u);
  const rsrc_t r_mb = make_rsrc(mrow + b * EB, EB * 8u);
  const rsrc_t r_o = make_rsrc(outp + b * EB, EB * 8u);

  ST(32); RT(50);
  const u32 tile = lane * 16 + wave;
  const u32* tab = (const u32*)(ws + WS_TAB) + (size_t)tile * 512;
  const u32 ent0 = tab[b], ent1 = tab[256 + b];
  const u32 mxw = ((const u32*)(ws + WS_MAX))[t & (NWG - 1)];
  u32 dv[4];
#pragma unroll
  for (int j = 0; j < 4; j++) dv[j] = __builtin_amdgcn_raw_buffer_load_b16(r_dist, (j * NTH + t) * 2u, 0, 0);
  const u32x2 ws0l = wcol(w_sbf, 0, 8, c4), ws0h = wcol(w_sbf, 4, 8, c4);
  const u32x2 ws1l = wcol(w_sbf, 0, 8, 4 + c4), ws1h = wcol(w_sbf, 4, 8, 4 + c4);
  const u32x2 wdc = wcol((const u16*)w_down, 0, 4, c4);

  Runs R = make_runs(ent0, tile);
  Burst B;
  load_burst(B, R, r_rec, 0, lane);
  auto pull = [&](int h) {
  };
  pull(0);

  if (t == 0) S.flag = 0;
  {
    const u32 mb = __builtin_amdgcn_readlane(wave_incl_max(mxw), 63);
    if (lane == 0) S.red[wave] = mb;
  }
#pragma unroll
  for (int j = 0; j < 4; j++) {
    const int i = j * NTH + t;
    if (j < 3 || wave == 0) {
      const u32x4 rb = edge_rbf(bf2f(dv[j]), centers);
      const f32x4 A0 = rb_times(rb, ws0l, ws0h), A1 = rb_times(rb, ws1l, ws1h);
      u32x4 a;
      u32 ke = 0;
#pragma unroll
      for (int d = 0; d < 4; d++) {
        const float n = fabsf(A0[d]) + fabsf(A1[d]);
        int e = __builtin_amdgcn_frexp_expf(n);
        e = e < -126 ? -126 : (e > 127 ? 127 : e);
        a[d] = pack_bf16(__builtin_amdgcn_ldexpf(A0[d], -e), __builtin_amdgcn_ldexpf(A1[d], -e));
        ke |= (u32)(e & 0xff) << (8 * d);
      }
      if (i < EP) {
        S.at[i] = i < EB ? a : (u32x4){0, 0, 0, 0};
        S.kexp[i] = ke;
        S.a01[i] = 0; S.a23[i] = 0; S.cnt[i] = 0;
      }
    }
    __builtin_amdgcn_sched_barrier(0);
  }
  ST(33);
  lds_barrier();
  ST(34); RT(52);

  u32 mall = umax(umax(S.red[0], S.red[1]), umax(S.red[2], S.red[3]));
  mall = __builtin_amdgcn_readfirstlane(mall);
  const float Bm = bf2f(mall);
  int ks[4];
  bool fixed_ok = true;
#pragma unroll
  for (int d = 0; d < 4; d++) {
    float cs = 0.f;
#pragma unroll
    for (int k = 0; k < 4; k++) cs += fabsf(wel(w_down, k * 4 + d));
    const float B = Bm * cs * 1.02f;
    const bool zero = (B == 0.f);
    if (!zero && !(B > 1e-25f && B < 1e25f)) fixed_ok = false;
    ks[d] = __builtin_amdgcn_readfirstlane(zero ? 0 : 22 - __builtin_amdgcn_frexp_expf(B));
  }
  Consts C;
  C.f0 = wel(freq, 0);
  C.f1 = wel(freq, 1);
  {
    const int kc = c4 == 0 ? ks[0] : (c4 == 1 ? ks[1] : (c4 == 2 ? ks[2] : ks[3]));
    C.wdT = (u32x2){pack_bf16(__builtin_amdgcn_ldexpf(bflo(wdc.x), kc), __builtin_amdgcn_ldexpf(bfhi(wdc.x), kc)),
                    pack_bf16(__builtin_amdgcn_ldexpf(bflo(wdc.y), kc), __builtin_amdgcn_ldexpf(bfhi(wdc.y), kc))};
  }

  bool use_float = !fixed_ok;
  if (!use_float) {
#pragma unroll 1
    for (int h = 0; h < 2; h++) {
      const rsrc_t r_m = make_rsrc(mrow + h * EH, EH * 8u);
      run_burst<0>(S, C, B, 0, R.total, r_m, lane);
      if (R.total > NW * 64) run_simple<0>(S, C, R, NW * 64, r_rec, r_m, lane);
      ST(35 + h);
      if (h == 0) {
        R = make_runs(ent1, tile);
        load_burst(B, R, r_rec, 0, lane);
        pull(1);
        lds_barrier();
      }
    }
    lds_barrier();
    ST(37); RT(53);
    u32 over = 0;
    for (int i = t; i < EB; i += NTH) over |= (S.cnt[i] >= 512u);
    if (over) S.flag = 1;
    lds_barrier();
    use_float = __builtin_amdgcn_readfirstlane(S.flag) != 0;
    if (use_float) {
      for (int i = t; i < EP; i += NTH) { S.a01[i] = 0; S.a23[i] = 0; }
      lds_barrier();
    }
  }
  if (use_float) {
    C.wdT = wdc;
#pragma unroll
    for (int d = 0; d < 4; d++) ks[d] = 0;
#pragma unroll 1
    for (int h = 0; h < 2; h++)
      run_simple<1>(S, C, make_runs(h ? ent1 : ent0, tile), 0, r_rec, make_rsrc(mrow + h * EH, EH * 8u), lane);
    lds_barrier();
  }

  ST(38);
  u32x2 mr[4];
#pragma unroll
  for (int j = 0; j < 4; j++) mr[j] = __builtin_amdgcn_raw_buffer_load_b64(r_mb, (j * NTH + t) * 8u, 0, 0);
  const u32x2 wrl = wcol(w_rbf, 0, 4, c4), wrh = wcol(w_rbf, 4, 4, c4);
#pragma unroll
  for (int j = 0; j < 4; j++) {
    const int i = j * NTH + t;
    if (j < 3 || wave == 0) {
      const int ii = i < EB ? i : 0;
      float ag[4];
      const u64 w01 = S.a01[ii], w23 = S.a23[ii];
      if (!use_float) {
        const u64 off = (u64)S.cnt[ii] * (((u64)MAGIC_BITS << 32) + MAGIC_BITS);
        const u64 v01 = w01 - off, v23 = w23 - off;
        const int l0 = (int)(u32)v01, l2 = (int)(u32)v23;
        const int h1 = (int)(u32)((v01 - (u64)(long long)l0) >> 32), h3 = (int)(u32)((v23 - (u64)(long long)l2) >> 32);
        ag[0] = (float)l0; ag[1] = (float)h1; ag[2] = (float)l2; ag[3] = (float)h3;
      } else {
        ag[0] = __uint_as_float((u32)w01); ag[1] = __uint_as_float((u32)(w01 >> 32));
        ag[2] = __uint_as_float((u32)w23); ag[3] = __uint_as_float((u32)(w23 >> 32));
      }
      const u32 ke = S.kexp[ii];
#pragma unroll
      for (int d = 0; d < 4; d++) ag[d] = __builtin_amdgcn_ldexpf(ag[d], (int)(signed char)(ke >> (8 * d)) - ks[d]);
      const u32x4 rb = edge_rbf(bf2f(dv[j]), centers);
      const f32x4 g = rb_times(rb, wrl, wrh);
      const float mv[4] = {bflo(mr[j].x), bfhi(mr[j].x), bflo(mr[j].y), bfhi(mr[j].y)};
      float o[4];
#pragma unroll
      for (int d = 0; d < 4; d++) {
        float u = 0.f;
#pragma unroll
        for (int k = 0; k < 4; k++) u += ag[k] * wel(w_up, k * 4 + d);
        o[d] = mv[d] + u * g[d];
      }
      __builtin_amdgcn_raw_buffer_store_b64((u32x2){pack_bf16(o[0], o[1]), pack_bf16(o[2], o[3])}, r_o, i < EB ? i * 8u : OOB, 0, 0);
    }
    __builtin_amdgcn_sched_barrier(0);
  }
  ST(39); RT(51);
}

extern "C" void kernel_launch(void* const* d_in, const int* in_sizes, int n_in, void* d_out, int out_size,
                              void* d_ws, size_t ws_size, hipStream_t stream) {
  const u32x2* m = (const u32x2*)d_in[0];
  const u32* idx_in = (const u32*)d_in[1];
  const u32* idx_out = (const u32*)d_in[2];
  const u16* dist = (const u16*)d_in[3];
  const u16* angle = (const u16*)d_in[4];
  const u32* centers = (const u32*)d_in[5];
  const u32* freq = (const u32*)d_in[6];
  const u16* w_rbf = (const u16*)d_in[7];
  const u16* w_sbf = (const u16*)d_in[8];
  const u32* w_down = (const u32*)d_in[9];
  const u32* w_up = (const u32*)d_in[10];
  k_part<<<NWG, NTH, 0, stream>>>(idx_in, idx_out, angle, m, (char*)d_ws);
  k_acc<<<NB, NTH, 0, stream>>>(m, dist, centers, freq, w_rbf, w_sbf, w_down, w_up, (const char*)d_ws, (u32x2*)d_out);
}
```

```cpp
#include <hip/hip_runtime.h>
#include <hip/hip_bf16.h>
#include <stdint.h>

typedef unsigned int u32;
typedef unsigned long long u64;
typedef unsigned short u16;
typedef u32 u32x2 __attribute__((ext_vector_type(2)));
typedef u32 u32x3 __attribute__((ext_vector_type(3)));
typedef u32 u32x4 __attribute__((ext_vector_type(4)));
typedef float f32x4 __attribute__((ext_vector_type(4)));
typedef short s16x4 __attribute__((ext_vector_type(4)));
typedef __bf16 bf16x2_t __attribute__((ext_vector_type(2)));
typedef float f32x2_t __attribute__((ext_vector_type(2)));

#define ABL 0
#define PREF 0
#define WS_OFF_MB 0
#define ST_AUX 16
#define REC_AUX 0

constexpr int E_ = 800000;
constexpr int T_ = 12800000;
constexpr int NB = 256;
constexpr int EB = 3125;
constexpr int EH = E_ / 2;
constexpr int NWG = 256;
constexpr int NTH = 1024;
constexpr int NT = 4;
constexpr int TILE = 12500;
constexpr int NTILES = NWG * NT;
constexpr int NSLOT = TILE + 512;
constexpr int TSTRIDE = 6528;
constexpr u32 NULLREC = (u32)EB;

constexpr size_t WS_TAB = 0;
constexpr size_t WS_MAX = WS_TAB + (size_t)NTILES * 512 * 4;
constexpr size_t WS_REC = WS_MAX + 4096;
static_assert(WS_REC % 128 == 0, "align");
static_assert(NSLOT / 2 <= TSTRIDE, "tile region");
static_assert(WS_REC + (size_t)NTILES * TSTRIDE * 12 <= (size_t)256 * 1024 * 1024, "ws");

__device__ __forceinline__ float bf2f(u32 h) { return __uint_as_float(h << 16); }
__device__ __forceinline__ float bflo(u32 w) { return __uint_as_float(w << 16); }
__device__ __forceinline__ float bfhi(u32 w) { return __uint_as_float(w & 0xffff0000u); }
__device__ __forceinline__ u32 pack_bf16(float a, float b) {
  f32x2_t v = {a, b};
  bf16x2_t r = __builtin_convertvector(v, bf16x2_t);
  return __builtin_bit_cast(u32, r);
}

__device__ __forceinline__ void lds_barrier() {
  asm volatile("s_waitcnt lgkmcnt(0)\n\ts_barrier" ::: "memory");
}

template <int CTRL, int RM>
__device__ __forceinline__ u32 dpp0(u32 v) { return (u32)__builtin_amdgcn_update_dpp(0, (int)v, CTRL, RM, 0xf, false); }
__device__ __forceinline__ u32 wave_incl_add(u32 v) {
  v += dpp0<0x111, 0xf>(v);
  v += dpp0<0x112, 0xf>(v);
  v += dpp0<0x114, 0xf>(v);
  v += dpp0<0x118, 0xf>(v);
  v += dpp0<0x142, 0xa>(v);
  v += dpp0<0x143, 0xc>(v);
  return v;
}
__device__ __forceinline__ u32 umax(u32 a, u32 b) { return a > b ? a : b; }
__device__ __forceinline__ u32 wave_incl_max(u32 v) {
  v = umax(v, dpp0<0x111, 0xf>(v));
  v = umax(v, dpp0<0x112, 0xf>(v));
  v = umax(v, dpp0<0x114, 0xf>(v));
  v = umax(v, dpp0<0x118, 0xf>(v));
  v = umax(v, dpp0<0x142, 0xa>(v));
  v = umax(v, dpp0<0x143, 0xc>(v));
  return v;
}

typedef __amdgpu_buffer_rsrc_t rsrc_t;
__device__ __forceinline__ rsrc_t make_rsrc(const void* p, u32 bytes) {
  return __builtin_amdgcn_make_buffer_rsrc((void*)p, 0, bytes, 0x00020000);
}
constexpr u32 OOB = 0x80000000u;
constexpr int AUX_NT = 2;

#define ST(i)
#define RT(i)

struct Raw { u32x4 in, out; u32x2 an; };

__global__ void __launch_bounds__(NTH) k_part(const u32* __restrict__ idx_in, const u32* __restrict__ idx_out,
                                              const u16* __restrict__ angle, const u32x2* __restrict__ mrow,
                                              char* __restrict__ ws) {
  __shared__ __attribute__((aligned(16))) u64 stage[NSLOT + 4];
  __shared__ __attribute__((aligned(16))) u32 hist[512];
  __shared__ __attribute__((aligned(16))) u32 cursor[512];
  __shared__ u32 sinfo[4];
  __shared__ u32 smax[16];

  const int t = threadIdx.x;
  const int lane = t & 63;
  const int wave = __builtin_amdgcn_readfirstlane(t >> 6);
  const int w = blockIdx.x;
  const rsrc_t r_in = make_rsrc(idx_in, T_ * 4u), r_out = make_rsrc(idx_out, T_ * 4u), r_an = make_rsrc(angle, T_ * 2u);

  Raw raw[3];
  u32 xin, xout, xan;
  constexpr int NX = TILE - 12 * NTH;
  const u32 vo16 = t * 16u, vo8 = t * 8u;
  const u32 xo4 = t < NX ? t * 4u : OOB, xo2 = t < NX ? t * 2u : OOB;
  auto load_slot = [&](int k, int s) {
    const u32 r = (u32)(w * NT + k) * TILE + s * NTH * 4;
    __builtin_amdgcn_sched_barrier(0);
    if (s < 3) {
      raw[s].in = __builtin_amdgcn_raw_buffer_load_b128(r_in, vo16, r * 4u, AUX_NT);
      raw[s].out = __builtin_amdgcn_raw_buffer_load_b128(r_out, vo16, r * 4u, AUX_NT);
      raw[s].an = __builtin_amdgcn_raw_buffer_load_b64(r_an, vo8, r * 2u, AUX_NT);
    } else {
      xin = __builtin_amdgcn_raw_buffer_load_b32(r_in, xo4, r * 4u, AUX_NT);
      xout = __builtin_amdgcn_raw_buffer_load_b32(r_out, xo4, r * 4u, AUX_NT);
      xan = __builtin_amdgcn_raw_buffer_load_b16(r_an, xo2, r * 2u, AUX_NT);
    }
    __builtin_amdgcn_sched_barrier(0);
  };
  u32 lo[13], hi[13];
  auto key = [&](u32 in, u32 out, u32 an16, u32& l, u32& h) {
    u32 q;
    asm("v_mul_hi_u32_u24 %0, %1, %2" : "=v"(q) : "v"(out), "v"(1374390u));
    const u32 loc = out - q * EB;
    const bool up = in >= (u32)EH;
    const u32 b4 = q * 4 + (up ? 1024u : 0u);
    l = ((in - (up ? (u32)EH : 0u)) << 12) | loc;
    h = an16 | (b4 << 16);
    return b4;
  };

  ST(0); RT(48);
  {
    const rsrc_t r_m = make_rsrc(mrow + w * EB, EB * 8u);
    u32x2 r[4];
#pragma unroll
    for (int i = 0; i < 4; i++) r[i] = __builtin_amdgcn_raw_buffer_load_b64(r_m, vo8, i * NTH * 8u, 0);
#pragma unroll
    for (int s = 0; s < 4; s++) load_slot(0, s);
    u32 mx = 0;
#pragma unroll
    for (int i = 0; i < 4; i++) {
      const u32 keep = (i * NTH + t < EB) ? 0x7fffu : 0u;
      mx = umax(mx, umax(umax(r[i].x & keep, (r[i].x >> 16) & keep), umax(r[i].y & keep, (r[i].y >> 16) & keep)));
    }
    mx = wave_incl_max(mx);
    if (lane == 63) smax[wave] = mx;
  }
  if (t < 512) hist[t] = 0;
  lds_barrier();
  if (t == 0) {
    u32 mx = 0;
#pragma unroll
    for (int i = 0; i < 16; i++) mx = umax(mx, smax[i]);
    ((u32*)(ws + WS_MAX))[w] = mx;
  }
  ST(1);

#pragma unroll
  for (int k = 0; k < NT; k++) {
    const int tile = w * NT + k;
#pragma unroll
    for (int s = 0; s < 3; s++) {
#pragma unroll
      for (int j = 0; j < 4; j++) {
        const u32 aw = raw[s].an[j >> 1];
        const u32 b4 = key(raw[s].in[j], raw[s].out[j], (j & 1) ? (aw >> 16) : (aw & 0xffffu), lo[s * 4 + j], hi[s * 4 + j]);
        atomicAdd((u32*)((char*)hist + b4), 8u);
      }
    }
    {
      const u32 b4 = key(xin, xout, xan, lo[12], hi[12]);
      if (t < NX) atomicAdd((u32*)((char*)hist + b4), 8u);
    }
    if (k + 1 < NT) load_slot(k + 1, 0);
    ST(2 + k * 6);
    lds_barrier();
    ST(3 + k * 6);
    u32x4 c0, c1;
    u32 run;
    if (wave == 1) {
      c0 = *(const u32x4*)&hist[lane * 8];
      c1 = *(const u32x4*)&hist[lane * 8 + 4];
      u32x4 p0 = (c0 + 8u) & ~15u, p1 = (c1 + 8u) & ~15u;
      const u32 tot = p0.x + p0.y + p0.z + p0.w + p1.x + p1.y + p1.z + p1.w;
      const u32 incl = wave_incl_add(tot);
      run = incl - tot;
      u32x4 a, b;
      a.x = run; a.y = a.x + p0.x; a.z = a.y + p0.y; a.w = a.z + p0.z;
      b.x = a.w + p0.w; b.y = b.x + p1.x; b.z = b.y + p1.y; b.w = b.z + p1.z;
      *(u32x4*)&cursor[lane * 8] = a;
      *(u32x4*)&cursor[lane * 8 + 4] = b;
      if (lane == 63) sinfo[0] = incl >> 4;
    }
    lds_barrier();
    ST(4 + k * 6);
    if (wave == 1) {
      *(u32x4*)&hist[lane * 8] = (u32x4){0, 0, 0, 0};
      *(u32x4*)&hist[lane * 8 + 4] = (u32x4){0, 0, 0, 0};
      const rsrc_t r_tab = make_rsrc(ws + WS_TAB + (size_t)tile * 2048, 2048);
#pragma unroll
      for (int h = 0; h < 2; h++) {
        const u32x4 c = h ? c1 : c0;
        u32x4 ent;
#pragma unroll
        for (int j = 0; j < 4; j++) {
          const u32 p = (c[j] + 8u) & ~15u;
          ent[j] = (run >> 4) | (p << 12);
          *(u64*)((char*)stage + ((c[j] & 8u) ? run + c[j] : (u32)(NSLOT * 8))) = (u64)NULLREC;
          run += p;
        }
        __builtin_amdgcn_raw_buffer_store_b128(ent, r_tab, lane * 32u + h * 16u, 0, 0);
        __builtin_amdgcn_sched_barrier(0);
      }
    }
    {
      u32 pos[13];
#pragma unroll
      for (int j = 0; j < 12; j++) pos[j] = atomicAdd((u32*)((char*)cursor + (hi[j] >> 16)), 8u);
      if (t < NX) pos[12] = atomicAdd((u32*)((char*)cursor + (hi[12] >> 16)), 8u);
      if (k + 1 < NT) load_slot(k + 1, 1);
#pragma unroll
      for (int j = 0; j < 12; j++) *(u64*)((char*)stage + pos[j]) = (u64)lo[j] | ((u64)hi[j] << 32);
      if (t < NX) *(u64*)((char*)stage + pos[12]) = (u64)lo[12] | ((u64)hi[12] << 32);
    }
    if (k + 1 < NT) { load_slot(k + 1, 2); load_slot(k + 1, 3); }
    ST(5 + k * 6);
    lds_barrier();
    ST(6 + k * 6);
    {
      const u32 nu = __builtin_amdgcn_readfirstlane(sinfo[0]);
      const rsrc_t r_rec = make_rsrc(ws + WS_REC + (size_t)tile * (TSTRIDE * 12), TSTRIDE * 12);
#pragma unroll
      for (int u = 0; u < 7; u++) {
        const u32 j = u * NTH + t;
        if (u < 6 || wave < 6) {
          const u32x4 q = *(const u32x4*)&stage[j < nu ? 2 * j : 0];
          const u32x3 v = {q.x, q.z, (q.y & 0xffffu) | (q.w << 16)};
          __builtin_amdgcn_raw_buffer_store_b96(v, r_rec, j < nu ? j * 12u : OOB, 0, ST_AUX);
        }
      }
    }
    ST(7 + k * 6);
  }
}

constexpr float MAGIC = 12582912.0f;
constexpr u32 MAGIC_BITS = 0x4B400000u;
constexpr int EP = EB + 3;

struct AccLds {
  u64 a01[EP];
  u64 a23[EP];
  u32x4 at[EP];
  u32 cnt[EP];
  u32 kexp[EP];
  u32 red[16];
  u32 flag;
};

struct Consts { u32x2 wdT; float f0, f1; };

__device__ __forceinline__ float dot2bf(u32 a, u32 b) {
  return __builtin_amdgcn_fdot2_f32_bf16(__builtin_bit_cast(bf16x2_t, a), __builtin_bit_cast(bf16x2_t, b), 0.f, false);
}

template <int MODE>
__device__ __forceinline__ void add_record(AccLds& S, const Consts& C, const f32x4& md, u32 loc, float ang) {
  const u32 pk = pack_bf16(C.f0 * ang, C.f1 * ang);
  const float INV2PI = 0.15915494309189535f;
  const float c0 = __builtin_amdgcn_cosf(__builtin_amdgcn_fractf(bflo(pk) * INV2PI));
  const float c1 = __builtin_amdgcn_cosf(__builtin_amdgcn_fractf(bfhi(pk) * INV2PI));
  const u32 cp = pack_bf16(c0, c1);
  const u32x4 a = S.at[loc];
  const float s0 = dot2bf(a.x, cp), s1 = dot2bf(a.y, cp), s2 = dot2bf(a.z, cp), s3 = dot2bf(a.w, cp);
  if (MODE == 0) {
    const u32 t0 = __float_as_uint(__builtin_fmaf(md.x, s0, MAGIC)), t1 = __float_as_uint(__builtin_fmaf(md.y, s1, MAGIC));
    const u32 t2 = __float_as_uint(__builtin_fmaf(md.z, s2, MAGIC)), t3 = __float_as_uint(__builtin_fmaf(md.w, s3, MAGIC));
    atomicAdd(&S.a01[loc], (u64)t0 | ((u64)t1 << 32));
    atomicAdd(&S.a23[loc], (u64)t2 | ((u64)t3 << 32));
    atomicAdd(&S.cnt[loc], 1u);
  } else {
    float* p01 = (float*)&S.a01[loc];
    float* p23 = (float*)&S.a23[loc];
    atomicAdd(p01, md.x * s0);
    atomicAdd(p01 + 1, md.y * s1);
    atomicAdd(p23, md.z * s2);
    atomicAdd(p23 + 1, md.w * s3);
  }
}

__device__ __forceinline__ f32x4 down(const Consts& C, const u32x2& row) {
  const f32x4 z = {0.f, 0.f, 0.f, 0.f};
  return __builtin_amdgcn_mfma_f32_4x4x4bf16_1k(__builtin_bit_cast(s16x4, C.wdT), __builtin_bit_cast(s16x4, row), z, 0, 0, 0);
}

struct Runs { u32 P4, base, total; };
__device__ __forceinline__ Runs make_runs(u32 ent, u32 tile) {
  Runs R;
  const u32 cnt = ent >> 16;
  const u32 incl = wave_incl_add(cnt);
  const u32 P = incl - cnt;
  R.total = __builtin_amdgcn_readlane(incl, 63);
  R.base = tile * TSTRIDE + (ent & 0xffffu) - P;
  R.P4 = cnt ? P * 4 : 0x7ffffff0u;
  return R;
}
__device__ __forceinline__ u32x3 load_unit(const Runs& R, rsrc_t r_rec, u32 u0, int lane) {
  const u32 d4 = R.P4 - u0 * 4;
  const bool part = d4 - 4u < 252u;
  u32 v = (u32)__builtin_amdgcn_ds_permute((int)(part ? d4 : 0u), (int)R.base);
  const u64 m0 = __ballot(R.P4 <= u0 * 4);
  const int r0 = 63 - __builtin_clzll(m0 | 1ull);
  const u32 b0 = __builtin_amdgcn_readlane(R.base, r0);
  v = lane == 0 ? b0 : v;
  v = wave_incl_max(v);
  const u32 u = u0 + lane;
  return __builtin_amdgcn_raw_buffer_load_b96(r_rec, u < R.total ? __umul24(v + u, 12u) : OOB, 0, REC_AUX);
}

constexpr int NW = 14;
struct Burst { u32x3 u[NW]; };
__device__ __forceinline__ void load_burst(Burst& B, const Runs& R, rsrc_t r_rec, u32 u0, int lane) {
#pragma unroll
  for (int j = 0; j < NW; j++) B.u[j] = load_unit(R, r_rec, u0 + j * 64, lane);
}

struct Win { u32x2 g0, g1; };
__device__ __forceinline__ void issue_win(Win& W, const u32x3& c, rsrc_t r_m, u32 u0, u32 total, int lane) {
  const bool v = u0 + lane < total;
  W.g0 = __builtin_amdgcn_raw_buffer_load_b64(r_m, v ? (c.x >> 9) & ~7u : OOB, 0, 0);
  W.g1 = __builtin_amdgcn_raw_buffer_load_b64(r_m, v ? (c.y >> 9) & ~7u : OOB, 0, 0);
}
template <int MODE>
__device__ __forceinline__ void proc_win(AccLds& S, const Consts& C, const Win& W, const u32x3& c, u32 u0, u32 total, int lane) {
  const f32x4 md0 = down(C, W.g0), md1 = down(C, W.g1);
  if (u0 + lane < total) {
    add_record<MODE>(S, C, md0, c.x & 0xfffu, bflo(c.z));
    add_record<MODE>(S, C, md1, c.y & 0xfffu, bfhi(c.z));
  }
}

#define GDEPTH 2
template <int MODE>
__device__ __forceinline__ void run_burst(AccLds& S, const Consts& C, const Burst& B, u32 u0, u32 total, rsrc_t r_m, int lane) {
  Win W[GDEPTH + 1];
#pragma unroll
  for (int j = 0; j < GDEPTH; j++) issue_win(W[j], B.u[j], r_m, u0 + j * 64, total, lane);
#pragma unroll
  for (int j = 0; j < NW; j++) {
    if (j + GDEPTH < NW) issue_win(W[(j + GDEPTH) % (GDEPTH + 1)], B.u[j + GDEPTH], r_m, u0 + (j + GDEPTH) * 64, total, lane);
    proc_win<MODE>(S, C, W[j % (GDEPTH + 1)], B.u[j], u0 + j * 64, total, lane);
  }
}

template <int MODE>
__device__ __forceinline__ void run_simple(AccLds& S, const Consts& C, const Runs& R, u32 u0, rsrc_t r_rec, rsrc_t r_m, int lane) {
#pragma unroll 1
  for (; u0 < R.total; u0 += 64) {
    const u32x3 c = load_unit(R, r_rec, u0, lane);
    Win W;
    issue_win(W, c, r_m, u0, R.total, lane);
    proc_win<MODE>(S, C, W, c, u0, R.total, lane);
  }
}

__device__ __forceinline__ float wel(const u32* __restrict__ w, int i) { return (i & 1) ? bfhi(w[i >> 1]) : bflo(w[i >> 1]); }

__device__ __forceinline__ u32x4 edge_rbf(float d, const u32* __restrict__ centers) {
  u32x4 rb;
#pragma unroll
  for (int r = 0; r < 4; r++) {
    u32 p = pack_bf16(d - wel(centers, 2 * r), d - wel(centers, 2 * r + 1));
    float x = bflo(p), y = bfhi(p);
    p = pack_bf16(x * x, y * y);
    p = pack_bf16(-10.0f * bflo(p), -10.0f * bfhi(p));
    rb[r] = pack_bf16(__expf(bflo(p)), __expf(bfhi(p)));
  }
  return rb;
}
__device__ __forceinline__ f32x4 rb_times(const u32x4& rb, const u32x2& wlo, const u32x2& whi) {
  f32x4 acc = {0.f, 0.f, 0.f, 0.f};
  acc = __builtin_amdgcn_mfma_f32_4x4x4bf16_1k(__builtin_bit_cast(s16x4, wlo), __builtin_bit_cast(s16x4, ((u32x2){rb.x, rb.y})), acc, 0, 0, 0);
  acc = __builtin_amdgcn_mfma_f32_4x4x4bf16_1k(__builtin_bit_cast(s16x4, whi), __builtin_bit_cast(s16x4, ((u32x2){rb.z, rb.w})), acc, 0, 0, 0);
  return acc;
}
__device__ __forceinline__ u32x2 wcol(const u16* __restrict__ w, int r0, int rs, int c) {
  return (u32x2){(u32)w[r0 * rs + c] | ((u32)w[(r0 + 1) * rs + c] << 16), (u32)w[(r0 + 2) * rs + c] | ((u32)w[(r0 + 3) * rs + c] << 16)};
}

__global__ void __launch_bounds__(NTH) k_acc(const u32x2* __restrict__ mrow, const u16* __restrict__ dist,
                                             const u32* __restrict__ centers, const u32* __restrict__ freq,
                                             const u16* __restrict__ w_rbf, const u16* __restrict__ w_sbf,
                                             const u32* __restrict__ w_down, const u32* __restrict__ w_up,
                                             const char* __restrict__ ws, u32x2* __restrict__ outp) {
  __shared__ __attribute__((aligned(16))) AccLds S;
  const int t = threadIdx.x;
  const int lane = t & 63;
  const int wave = __builtin_amdgcn_readfirstlane(t >> 6);
  const int b = (blockIdx.x & 7) * 32 + (blockIdx.x >> 3);
  const int c4 = lane & 3;
  const rsrc_t r_rec = make_rsrc(ws + WS_REC, (u32)NTILES * TSTRIDE * 12u);
  const rsrc_t r_dist = make_rsrc(dist + b * EB, EB * 2u);
  const rsrc_t r_mb = make_rsrc(mrow + b * EB, EB * 8u);
  const rsrc_t r_o = make_rsrc(outp + b * EB, EB * 8u);

  ST(32); RT(50);
  const u32 tile = lane * 16 + wave;
  const u32* tab = (const u32*)(ws + WS_TAB) + (size_t)tile * 512;
  const u32 ent0 = tab[b], ent1 = tab[256 + b];
  const u32 mxw = ((const u32*)(ws + WS_MAX))[t & (NWG - 1)];
  u32 dv[4];
#pragma unroll
  for (int j = 0; j < 4; j++) dv[j] = __builtin_amdgcn_raw_buffer_load_b16(r_dist, (j * NTH + t) * 2u, 0, 0);
  const u32x2 ws0l = wcol(w_sbf, 0, 8, c4), ws0h = wcol(w_sbf, 4, 8, c4);
  const u32x2 ws1l = wcol(w_sbf, 0, 8, 4 + c4), ws1h = wcol(w_sbf, 4, 8, 4 + c4);
  const u32x2 wdc = wcol((const u16*)w_down, 0, 4, c4);

  Runs R = make_runs(ent0, tile);
  Burst B;
  load_burst(B, R, r_rec, 0, lane);
  auto pull = [&](int h) {
  };
  pull(0);

  if (t == 0) S.flag = 0;
  {
    const u32 mb = __builtin_amdgcn_readlane(wave_incl_max(mxw), 63);
    if (lane == 0) S.red[wave] = mb;
  }
#pragma unroll
  for (int j = 0; j < 4; j++) {
    const int i = j * NTH + t;
    if (j < 3 || wave == 0) {
      const u32x4 rb = edge_rbf(bf2f(dv[j]), centers);
      const f32x4 A0 = rb_times(rb, ws0l, ws0h), A1 = rb_times(rb, ws1l, ws1h);
      u32x4 a;
      u32 ke = 0;
#pragma unroll
      for (int d = 0; d < 4; d++) {
        const float n = fabsf(A0[d]) + fabsf(A1[d]);
        int e = __builtin_amdgcn_frexp_expf(n);
        e = e < -126 ? -126 : (e > 127 ? 127 : e);
        a[d] = pack_bf16(__builtin_amdgcn_ldexpf(A0[d], -e), __builtin_amdgcn_ldexpf(A1[d], -e));
        ke |= (u32)(e & 0xff) << (8 * d);
      }
      if (i < EP) {
        S.at[i] = i < EB ? a : (u32x4){0, 0, 0, 0};
        S.kexp[i] = ke;
        S.a01[i] = 0; S.a23[i] = 0; S.cnt[i] = 0;
      }
    }
    __builtin_amdgcn_sched_barrier(0);
  }
  ST(33);
  lds_barrier();
  ST(34); RT(52);

  u32 mall = umax(umax(S.red[0], S.red[1]), umax(S.red[2], S.red[3]));
  mall = __builtin_amdgcn_readfirstlane(mall);
  const float Bm = bf2f(mall);
  int ks[4];
  bool fixed_ok = true;
#pragma unroll
  for (int d = 0; d < 4; d++) {
    float cs = 0.f;
#pragma unroll
    for (int k = 0; k < 4; k++) cs += fabsf(wel(w_down, k * 4 + d));
    const float B = Bm * cs * 1.02f;
    const bool zero = (B == 0.f);
    if (!zero && !(B > 1e-25f && B < 1e25f)) fixed_ok = false;
    ks[d] = __builtin_amdgcn_readfirstlane(zero ? 0 : 22 - __builtin_amdgcn_frexp_expf(B));
  }
  Consts C;
  C.f0 = wel(freq, 0);
  C.f1 = wel(freq, 1);
  {
    const int kc = c4 == 0 ? ks[0] : (c4 == 1 ? ks[1] : (c4 == 2 ? ks[2] : ks[3]));
    C.wdT = (u32x2){pack_bf16(__builtin_amdgcn_ldexpf(bflo(wdc.x), kc), __builtin_amdgcn_ldexpf(bfhi(wdc.x), kc)),
                    pack_bf16(__builtin_amdgcn_ldexpf(bflo(wdc.y), kc), __builtin_amdgcn_ldexpf(bfhi(wdc.y), kc))};
  }

  bool use_float = !fixed_ok;
  if (!use_float) {
#pragma unroll 1
    for (int h = 0; h < 2; h++) {
      const rsrc_t r_m = make_rsrc(mrow + h * EH, EH * 8u);
      run_burst<0>(S, C, B, 0, R.total, r_m, lane);
      if (R.total > NW * 64) run_simple<0>(S, C, R, NW * 64, r_rec, r_m, lane);
      ST(35 + h);
      if (h == 0) {
        R = make_runs(ent1, tile);
        load_burst(B, R, r_rec, 0, lane);
        pull(1);
        lds_barrier();
      }
    }
    lds_barrier();
    ST(37); RT(53);
    u32 over = 0;
    for (int i = t; i < EB; i += NTH) over |= (S.cnt[i] >= 512u);
    if (over) S.flag = 1;
    lds_barrier();
    use_float = __builtin_amdgcn_readfirstlane(S.flag) != 0;
    if (use_float) {
      for (int i = t; i < EP; i += NTH) { S.a01[i] = 0; S.a23[i] = 0; }
      lds_barrier();
    }
  }
  if (use_float) {
    C.wdT = wdc;
#pragma unroll
    for (int d = 0; d < 4; d++) ks[d] = 0;
#pragma unroll 1
    for (int h = 0; h < 2; h++)
      run_simple<1>(S, C, make_runs(h ? ent1 : ent0, tile), 0, r_rec, make_rsrc(mrow + h * EH, EH * 8u), lane);
    lds_barrier();
  }

  ST(38);
  u32x2 mr[4];
#pragma unroll
  for (int j = 0; j < 4; j++) mr[j] = __builtin_amdgcn_raw_buffer_load_b64(r_mb, (j * NTH + t) * 8u, 0, 0);
  const u32x2 wrl = wcol(w_rbf, 0, 4, c4), wrh = wcol(w_rbf, 4, 4, c4);
#pragma unroll
  for (int j = 0; j < 4; j++) {
    const int i = j * NTH + t;
    if (j < 3 || wave == 0) {
      const int ii = i < EB ? i : 0;
      float ag[4];
      const u64 w01 = S.a01[ii], w23 = S.a23[ii];
      if (!use_float) {
        const u64 off = (u64)S.cnt[ii] * (((u64)MAGIC_BITS << 32) + MAGIC_BITS);
        const u64 v01 = w01 - off, v23 = w23 - off;
        const int l0 = (int)(u32)v01, l2 = (int)(u32)v23;
        const int h1 = (int)(u32)((v01 - (u64)(long long)l0) >> 32), h3 = (int)(u32)((v23 - (u64)(long long)l2) >> 32);
        ag[0] = (float)l0; ag[1] = (float)h1; ag[2] = (float)l2; ag[3] = (float)h3;
      } else {
        ag[0] = __uint_as_float((u32)w01); ag[1] = __uint_as_float((u32)(w01 >> 32));
        ag[2] = __uint_as_float((u32)w23); ag[3] = __uint_as_float((u32)(w23 >> 32));
      }
      const u32 ke = S.kexp[ii];
#pragma unroll
      for (int d = 0; d < 4; d++) ag[d] = __builtin_amdgcn_ldexpf(ag[d], (int)(signed char)(ke >> (8 * d)) - ks[d]);
      const u32x4 rb = edge_rbf(bf2f(dv[j]), centers);
      const f32x4 g = rb_times(rb, wrl, wrh);
      const float mv[4] = {bflo(mr[j].x), bfhi(mr[j].x), bflo(mr[j].y), bfhi(mr[j].y)};
      float o[4];
#pragma unroll
      for (int d = 0; d < 4; d++) {
        float u = 0.f;
#pragma unroll
        for (int k = 0; k < 4; k++) u += ag[k] * wel(w_up, k * 4 + d);
        o[d] = mv[d] + u * g[d];
      }
      __builtin_amdgcn_raw_buffer_store_b64((u32x2){pack_bf16(o[0], o[1]), pack_bf16(o[2], o[3])}, r_o, i < EB ? i * 8u : OOB, 0, 0);
    }
    __builtin_amdgcn_sched_barrier(0);
  }
  ST(39); RT(51);
}

extern "C" void kernel_launch(void* const* d_in, const int* in_sizes, int n_in, void* d_out, int out_size,
                              void* d_ws, size_t ws_size, hipStream_t stream) {
  const u32x2* m = (const u32x2*)d_in[0];
  const u32* idx_in = (const u32*)d_in[1];
  const u32* idx_out = (const u32*)d_in[2];
  const u16* dist = (const u16*)d_in[3];
  const u16* angle = (const u16*)d_in[4];
  const u32* centers = (const u32*)d_in[5];
  const u32* freq = (const u32*)d_in[6];
  const u16* w_rbf = (const u16*)d_in[7];
  const u16* w_sbf = (const u16*)d_in[8];
  const u32* w_down = (const u32*)d_in[9];
  const u32* w_up = (const u32*)d_in[10];
  char* ws = (char*)d_ws + ((size_t)WS_OFF_MB << 20);
  k_part<<<NWG, NTH, 0, stream>>>(idx_in, idx_out, angle, m, ws);
  k_acc<<<NB, NTH, 0, stream>>>(m, dist, centers, freq, w_rbf, w_sbf, w_down, w_up, (const char*)ws, (u32x2*)d_out);
}
```

```cpp
#include <hip/hip_runtime.h>
#include <hip/hip_bf16.h>
#include <stdint.h>

typedef unsigned int u32;
typedef unsigned long long u64;
typedef unsigned short u16;
typedef u32 u32x2 __attribute__((ext_vector_type(2)));
typedef u32 u32x3 __attribute__((ext_vector_type(3)));
typedef u32 u32x4 __attribute__((ext_vector_type(4)));
typedef float f32x4 __attribute__((ext_vector_type(4)));
typedef short s16x4 __attribute__((ext_vector_type(4)));
typedef __bf16 bf16x2_t __attribute__((ext_vector_type(2)));
typedef float f32x2_t __attribute__((ext_vector_type(2)));

#define ABL 0
#define PREF 0
#define WS_OFF_MB 0
#define PRIO 1
#define LATE 0
#define BAR0 0
#define GFIRST 0
#ifndef M_AUX
#define M_AUX 0
#endif
#define EARLY2 0
#define ST_AUX 16
#define REC_AUX 0

constexpr int E_ = 800000;
constexpr int T_ = 12800000;
constexpr int NB = 256;
constexpr int EB = 3125;
constexpr int EH = E_ / 2;
constexpr int NWG = 256;
constexpr int NTH = 1024;
constexpr int NT = 4;
constexpr int TILE = 12500;
constexpr int NTILES = NWG * NT;
constexpr int NSLOT = TILE + 512;
constexpr int TSTRIDE = 6528;
constexpr u32 NULLREC = (u32)EB;

constexpr int TABS = 544;
constexpr size_t WS_TAB = 0;
constexpr size_t WS_MAX = WS_TAB + (size_t)NTILES * TABS * 4;
constexpr size_t WS_REC = WS_MAX + 4096;
static_assert(WS_REC % 128 == 0, "align");
static_assert(NSLOT / 2 <= TSTRIDE, "tile region");
static_assert(WS_REC + (size_t)NTILES * TSTRIDE * 12 <= (size_t)256 * 1024 * 1024, "ws");

__device__ __forceinline__ float bf2f(u32 h) { return __uint_as_float(h << 16); }
__device__ __forceinline__ float bflo(u32 w) { return __uint_as_float(w << 16); }
__device__ __forceinline__ float bfhi(u32 w) { return __uint_as_float(w & 0xffff0000u); }
__device__ __forceinline__ u32 pack_bf16(float a, float b) {
  f32x2_t v = {a, b};
  bf16x2_t r = __builtin_convertvector(v, bf16x2_t);
  return __builtin_bit_cast(u32, r);
}

__device__ __forceinline__ void lds_barrier() {
  asm volatile("s_waitcnt lgkmcnt(0)\n\ts_barrier" ::: "memory");
}

template <int CTRL, int RM>
__device__ __forceinline__ u32 dpp0(u32 v) { return (u32)__builtin_amdgcn_update_dpp(0, (int)v, CTRL, RM, 0xf, false); }
__device__ __forceinline__ u32 wave_incl_add(u32 v) {
  v += dpp0<0x111, 0xf>(v);
  v += dpp0<0x112, 0xf>(v);
  v += dpp0<0x114, 0xf>(v);
  v += dpp0<0x118, 0xf>(v);
  v += dpp0<0x142, 0xa>(v);
  v += dpp0<0x143, 0xc>(v);
  return v;
}
__device__ __forceinline__ u32 umax(u32 a, u32 b) { return a > b ? a : b; }
__device__ __forceinline__ u32 wave_incl_max(u32 v) {
  v = umax(v, dpp0<0x111, 0xf>(v));
  v = umax(v, dpp0<0x112, 0xf>(v));
  v = umax(v, dpp0<0x114, 0xf>(v));
  v = umax(v, dpp0<0x118, 0xf>(v));
  v = umax(v, dpp0<0x142, 0xa>(v));
  v = umax(v, dpp0<0x143, 0xc>(v));
  return v;
}

typedef __amdgpu_buffer_rsrc_t rsrc_t;
__device__ __forceinline__ rsrc_t make_rsrc(const void* p, u32 bytes) {
  return __builtin_amdgcn_make_buffer_rsrc((void*)p, 0, bytes, 0x00020000);
}
constexpr u32 OOB = 0x80000000u;
constexpr int AUX_NT = 2;

#define ST(i)
#define RT(i)
#define SP(i)

struct Raw { u32x4 in, out; u32x2 an; };

__global__ void __launch_bounds__(NTH) k_part(const u32* __restrict__ idx_in, const u32* __restrict__ idx_out,
                                              const u16* __restrict__ angle, const u32x2* __restrict__ mrow,
                                              char* __restrict__ ws) {
  __shared__ __attribute__((aligned(16))) u64 stage[NSLOT + 4];
  __shared__ __attribute__((aligned(16))) u32 hist[512];
  __shared__ __attribute__((aligned(16))) u32 cursor[512];
  __shared__ u32 sinfo[4];
  __shared__ u32 smax[16];

  const int t = threadIdx.x;
  const int lane = t & 63;
  const int wave = __builtin_amdgcn_readfirstlane(t >> 6);
  const int w = blockIdx.x;
  const rsrc_t r_in = make_rsrc(idx_in, T_ * 4u), r_out = make_rsrc(idx_out, T_ * 4u), r_an = make_rsrc(angle, T_ * 2u);

  Raw raw[3];
  u32 xin, xout, xan;
  constexpr int NX = TILE - 12 * NTH;
  auto load_slot = [&](int k, int s) {
    const u32 r = (u32)(w * NT + k) * TILE + s * NTH * 4;
    const bool live = k < NT;
    __builtin_amdgcn_sched_barrier(0);
    if (s < 3) {
      const u32 vo16 = live ? t * 16u : OOB, vo8 = live ? t * 8u : OOB;
      raw[s].in = __builtin_amdgcn_raw_buffer_load_b128(r_in, vo16, r * 4u, AUX_NT);
      raw[s].out = __builtin_amdgcn_raw_buffer_load_b128(r_out, vo16, r * 4u, AUX_NT);
      raw[s].an = __builtin_amdgcn_raw_buffer_load_b64(r_an, vo8, r * 2u, AUX_NT);
    } else {
      const u32 xo4 = (live && t < NX) ? t * 4u : OOB, xo2 = (live && t < NX) ? t * 2u : OOB;
      xin = __builtin_amdgcn_raw_buffer_load_b32(r_in, xo4, r * 4u, AUX_NT);
      xout = __builtin_amdgcn_raw_buffer_load_b32(r_out, xo4, r * 4u, AUX_NT);
      xan = __builtin_amdgcn_raw_buffer_load_b16(r_an, xo2, r * 2u, AUX_NT);
    }
    __builtin_amdgcn_sched_barrier(0);
  };
  u32 lo[13], hi[13];
  auto key = [&](u32 in, u32 out, u32 an16, u32& l, u32& h) {
    u32 q;
    asm("v_mul_hi_u32_u24 %0, %1, %2" : "=v"(q) : "v"(out), "v"(1374390u));
    const u32 loc = out - q * EB;
    const bool up = in >= (u32)EH;
    const u32 b4 = q * 4 + (up ? 1024u : 0u);
    l = ((in - (up ? (u32)EH : 0u)) << 12) | loc;
    h = an16 | (b4 << 16);
    return b4;
  };

  ST(0); RT(48);
  {
    const rsrc_t r_m = make_rsrc(mrow + w * EB, EB * 8u);
    u32x2 r[4];
#pragma unroll
    for (int i = 0; i < 4; i++) r[i] = __builtin_amdgcn_raw_buffer_load_b64(r_m, t * 8u, i * NTH * 8u, M_AUX);
#pragma unroll
    for (int s = 0; s < 4; s++) load_slot(0, s);
    if (t < 512) hist[t] = 0;
    u32 mx = 0;
#pragma unroll
    for (int i = 0; i < 4; i++) {
      const u32 keep = (i * NTH + t < EB) ? 0x7fffu : 0u;
      mx = umax(mx, umax(umax(r[i].x & keep, (r[i].x >> 16) & keep), umax(r[i].y & keep, (r[i].y >> 16) & keep)));
    }
    mx = wave_incl_max(mx);
    if (lane == 63) smax[wave] = mx;
  }
  lds_barrier();
  if (t == 0) {
    u32 mx = 0;
#pragma unroll
    for (int i = 0; i < 16; i++) mx = umax(mx, smax[i]);
    ((u32*)(ws + WS_MAX))[w] = mx;
  }
  ST(1);

#pragma unroll 1
  for (int k = 0; k < NT; k++) {
    const int tile = w * NT + k;
#pragma unroll
    for (int s = 0; s < 3; s++) {
#pragma unroll
      for (int j = 0; j < 4; j++) {
        const u32 aw = raw[s].an[j >> 1];
        const u32 b4 = key(raw[s].in[j], raw[s].out[j], (j & 1) ? (aw >> 16) : (aw & 0xffffu), lo[s * 4 + j], hi[s * 4 + j]);
        atomicAdd((u32*)((char*)hist + b4), 8u);
      }
    }
    {
      const u32 b4 = key(xin, xout, xan, lo[12], hi[12]);
      if (t < NX) atomicAdd((u32*)((char*)hist + b4), 8u);
    }
    load_slot(k + 1, 0);
    ST(2 + k * 6);
    lds_barrier();
    ST(3 + k * 6);
    u32x4 c0, c1;
    u32 run;
    if (wave == 1) {
      c0 = *(const u32x4*)&hist[lane * 8];
      c1 = *(const u32x4*)&hist[lane * 8 + 4];
      u32x4 p0 = (c0 + 8u) & ~15u, p1 = (c1 + 8u) & ~15u;
      const u32 tot = p0.x + p0.y + p0.z + p0.w + p1.x + p1.y + p1.z + p1.w;
      const u32 incl = wave_incl_add(tot);
      run = incl - tot;
      u32x4 a, b;
      a.x = run; a.y = a.x + p0.x; a.z = a.y + p0.y; a.w = a.z + p0.z;
      b.x = a.w + p0.w; b.y = b.x + p1.x; b.z = b.y + p1.y; b.w = b.z + p1.z;
      *(u32x4*)&cursor[lane * 8] = a;
      *(u32x4*)&cursor[lane * 8 + 4] = b;
      if (lane == 63) sinfo[0] = incl >> 4;
    }
    lds_barrier();
    ST(4 + k * 6);
    if (wave == 1) {
      *(u32x4*)&hist[lane * 8] = (u32x4){0, 0, 0, 0};
      *(u32x4*)&hist[lane * 8 + 4] = (u32x4){0, 0, 0, 0};
      const rsrc_t r_tab = make_rsrc(ws + WS_TAB + (size_t)tile * (TABS * 4), 2048);
#pragma unroll
      for (int h = 0; h < 2; h++) {
        const u32x4 c = h ? c1 : c0;
        u32x4 ent;
#pragma unroll
        for (int j = 0; j < 4; j++) {
          const u32 p = (c[j] + 8u) & ~15u;
          ent[j] = (run >> 4) | (p << 12);
          *(u64*)((char*)stage + ((c[j] & 8u) ? run + c[j] : (u32)(NSLOT * 8))) = (u64)NULLREC;
          run += p;
        }
        __builtin_amdgcn_raw_buffer_store_b128(ent, r_tab, lane * 32u + h * 16u, 0, 0);
        __builtin_amdgcn_sched_barrier(0);
      }
    }
    {
      u32 pos[13];
#pragma unroll
      for (int j = 0; j < 12; j++) pos[j] = atomicAdd((u32*)((char*)cursor + (hi[j] >> 16)), 8u);
      if (t < NX) pos[12] = atomicAdd((u32*)((char*)cursor + (hi[12] >> 16)), 8u);
      load_slot(k + 1, 1);
#pragma unroll
      for (int j = 0; j < 12; j++) *(u64*)((char*)stage + pos[j]) = (u64)lo[j] | ((u64)hi[j] << 32);
      if (t < NX) *(u64*)((char*)stage + pos[12]) = (u64)lo[12] | ((u64)hi[12] << 32);
    }
    load_slot(k + 1, 2);
    load_slot(k + 1, 3);
    ST(5 + k * 6);
    lds_barrier();
    ST(6 + k * 6);
    {
      const u32 nu = __builtin_amdgcn_readfirstlane(sinfo[0]);
      const rsrc_t r_rec = make_rsrc(ws + WS_REC + (size_t)tile * (TSTRIDE * 12), TSTRIDE * 12);
#pragma unroll
      for (int u = 0; u < 7; u++) {
        const u32 j = u * NTH + t;
        const u32x4 q = *(const u32x4*)&stage[j < nu ? 2 * j : 0];
        const u32x3 v = {q.x, __builtin_amdgcn_perm(q.w, q.y, 0x05040100u), q.z};
        __builtin_amdgcn_raw_buffer_store_b96(v, r_rec, j < nu ? j * 12u : OOB, 0, ST_AUX);
      }
    }
    ST(7 + k * 6);
  }
}

constexpr float MAGIC = 12582912.0f;
constexpr u32 MAGIC_BITS = 0x4B400000u;
constexpr int EP = EB + 3;
constexpr int NW = 14;
#define GDEPTH 2

struct AccLds {
  u64 a01[EP];
  u64 a23[EP];
  u32x4 at[EP];
  u32 cnt[EP];
  u64 bmap[16][16];
  u32 red[16];
  u32 flag;
};

struct Consts { u32x2 wdT; float f0, f1; };

__device__ __forceinline__ float dot2bf(u32 a, u32 b) {
  return __builtin_amdgcn_fdot2_f32_bf16(__builtin_bit_cast(bf16x2_t, a), __builtin_bit_cast(bf16x2_t, b), 0.f, false);
}

template <int MODE>
__device__ __forceinline__ void add_record(AccLds& S, const Consts& C, const f32x4& md, u32 loc, float ang) {
  const u32 pk = pack_bf16(C.f0 * ang, C.f1 * ang);
  const float INV2PI = 0.15915494309189535f;
  const float c0 = __builtin_amdgcn_cosf(__builtin_amdgcn_fractf(bflo(pk) * INV2PI));
  const float c1 = __builtin_amdgcn_cosf(__builtin_amdgcn_fractf(bfhi(pk) * INV2PI));
  const u32 cp = pack_bf16(c0, c1);
  const u32x4 a = S.at[loc];
  const float s0 = dot2bf(a.x, cp), s1 = dot2bf(a.y, cp), s2 = dot2bf(a.z, cp), s3 = dot2bf(a.w, cp);
  if (MODE == 0) {
    const u32 t0 = __float_as_uint(__builtin_fmaf(md.x, s0, MAGIC)), t1 = __float_as_uint(__builtin_fmaf(md.y, s1, MAGIC));
    const u32 t2 = __float_as_uint(__builtin_fmaf(md.z, s2, MAGIC)), t3 = __float_as_uint(__builtin_fmaf(md.w, s3, MAGIC));
    atomicAdd(&S.a01[loc], (u64)t0 | ((u64)t1 << 32));
    atomicAdd(&S.a23[loc], (u64)t2 | ((u64)t3 << 32));
    atomicAdd(&S.cnt[loc], 1u);
  } else {
    float* p01 = (float*)&S.a01[loc];
    float* p23 = (float*)&S.a23[loc];
    atomicAdd(p01, md.x * s0);
    atomicAdd(p01 + 1, md.y * s1);
    atomicAdd(p23, md.z * s2);
    atomicAdd(p23 + 1, md.w * s3);
  }
}

__device__ __forceinline__ f32x4 mat4(const u32x2& wT, const u32x2& row, const f32x4& acc) {
  return __builtin_amdgcn_mfma_f32_4x4x4bf16_1k(__builtin_bit_cast(s16x4, wT), __builtin_bit_cast(s16x4, row), acc, 0, 0, 0);
}

struct Runs { u32 cnt, P, base, total; };
__device__ __forceinline__ Runs make_runs(u32 ent, u32 tile) {
  Runs R;
  R.cnt = ent >> 16;
  const u32 incl = wave_incl_add(R.cnt);
  R.P = incl - R.cnt;
  R.total = __builtin_amdgcn_readlane(incl, 63);
  R.base = tile * TSTRIDE + (ent & 0xffffu) - R.P;
  return R;
}

struct Burst { u32x3 u[NW]; };
__device__ __forceinline__ void map_burst(u32 (&off)[NW], const Runs& R, u64* bm, int lane) {
  const bool ne = R.cnt != 0;
  const u64 nz = __ballot(ne);
  const u32 rank = __builtin_amdgcn_mbcnt_hi((u32)(nz >> 32), __builtin_amdgcn_mbcnt_lo((u32)nz, 0));
  const u32 cbase = (u32)__builtin_amdgcn_ds_permute((int)(ne ? rank * 4 : 252u), (int)R.base);
  if (lane < 16) bm[lane] = 0;
  if (ne && R.P < NW * 64) atomicOr((u32*)bm + (R.P >> 5), 1u << (R.P & 31));
  const u64 word = bm[lane & 15];
  const u32 wlo = (u32)word, whi = (u32)(word >> 32);
  u32 cum = 0;
#pragma unroll
  for (int j = 0; j < NW; j++) {
    const u64 M = (u64)(u32)__builtin_amdgcn_readlane(wlo, j) | ((u64)(u32)__builtin_amdgcn_readlane(whi, j) << 32);
    const u32 r0 = cum + (u32)(M & 1) - 1;
    const u64 M1 = M >> 1;
    const u32 r = __builtin_amdgcn_mbcnt_hi((u32)(M1 >> 32), __builtin_amdgcn_mbcnt_lo((u32)M1, r0));
    cum += __builtin_popcountll(M);
    const u32 u = j * 64 + lane;
    const u32 v = (u32)__builtin_amdgcn_ds_bpermute((int)(r * 4), (int)cbase);
    off[j] = u < R.total ? __umul24(v + u, 12u) : OOB;
  }
}
template <int J0, int J1>
__device__ __forceinline__ void load_burst(Burst& B, const u32 (&off)[NW], rsrc_t r_rec) {
  __builtin_amdgcn_sched_barrier(0);
#pragma unroll
  for (int j = J0; j < J1; j++) B.u[j] = __builtin_amdgcn_raw_buffer_load_b96(r_rec, off[j], 0, REC_AUX);
  __builtin_amdgcn_sched_barrier(0);
}

struct Win { u32x2 g0, g1; };
__device__ __forceinline__ void issue_win(Win& W, const u32x3& c, rsrc_t r_m, u32 u0, u32 total, int lane) {
  const bool v = u0 + lane < total;
  W.g0 = __builtin_amdgcn_raw_buffer_load_b64(r_m, v ? (c.x >> 9) & ~7u : OOB, 0, 0);
  W.g1 = __builtin_amdgcn_raw_buffer_load_b64(r_m, v ? (c.z >> 9) & ~7u : OOB, 0, 0);
}
template <int MODE>
__device__ __forceinline__ void proc_win(AccLds& S, const Consts& C, const Win& W, const u32x3& c, u32 u0, u32 total, int lane) {
  const f32x4 z = {0.f, 0.f, 0.f, 0.f};
  const f32x4 md0 = mat4(C.wdT, W.g0, z), md1 = mat4(C.wdT, W.g1, z);
  if (u0 + lane < total) {
    add_record<MODE>(S, C, md0, c.x & 0xfffu, bflo(c.y));
    add_record<MODE>(S, C, md1, c.z & 0xfffu, bfhi(c.y));
  }
}

struct Pipe { Win W[GDEPTH + 1]; };
__device__ __forceinline__ void start_burst(Pipe& Q, const Burst& B, u32 total, rsrc_t r_m, int lane) {
#pragma unroll
  for (int j = 0; j < GDEPTH; j++) issue_win(Q.W[j], B.u[j], r_m, j * 64, total, lane);
}
__device__ __forceinline__ void run_burst(AccLds& S, const Consts& C, Pipe& Q, const Burst& B, u32 total, rsrc_t r_m, int lane) {
#pragma unroll
  for (int j = 0; j < NW; j++) {
    if (j + GDEPTH < NW) issue_win(Q.W[(j + GDEPTH) % (GDEPTH + 1)], B.u[j + GDEPTH], r_m, (j + GDEPTH) * 64, total, lane);
    proc_win<0>(S, C, Q.W[j % (GDEPTH + 1)], B.u[j], j * 64, total, lane);
  }
}

template <int MODE>
__device__ __forceinline__ void run_simple(AccLds& S, const Consts& C, const Runs& R, u32 u0, rsrc_t r_rec, rsrc_t r_m, int lane) {
#pragma unroll 1
  for (; u0 < R.total; u0 += 64) {
    const u32 d = R.P - u0;
    const bool part = R.cnt != 0 && d - 1u < 63u;
    u32 v = (u32)__builtin_amdgcn_ds_permute((int)(part ? d * 4 : 0u), (int)R.base);
    const u64 m0 = __ballot(R.cnt != 0 && R.P <= u0);
    const u32 b0 = __builtin_amdgcn_readlane(R.base, 63 - __builtin_clzll(m0 | 1ull));
    v = wave_incl_max(lane == 0 ? b0 : v);
    const u32 u = u0 + lane;
    const u32x3 c = __builtin_amdgcn_raw_buffer_load_b96(r_rec, u < R.total ? __umul24(v + u, 12u) : OOB, 0, 0);
    Win W;
    issue_win(W, c, r_m, u0, R.total, lane);
    proc_win<MODE>(S, C, W, c, u0, R.total, lane);
  }
}

__device__ __forceinline__ float wel(const u32* __restrict__ w, int i) { return (i & 1) ? bfhi(w[i >> 1]) : bflo(w[i >> 1]); }

__device__ __forceinline__ u32x4 edge_rbf(float d, const u32* __restrict__ centers) {
  u32x4 rb;
#pragma unroll
  for (int r = 0; r < 4; r++) {
    u32 p = pack_bf16(d - wel(centers, 2 * r), d - wel(centers, 2 * r + 1));
    float x = bflo(p), y = bfhi(p);
    p = pack_bf16(x * x, y * y);
    p = pack_bf16(-10.0f * bflo(p), -10.0f * bfhi(p));
    rb[r] = pack_bf16(__expf(bflo(p)), __expf(bfhi(p)));
  }
  return rb;
}
__device__ __forceinline__ f32x4 rb_times(const u32x4& rb, const u32x2& wlo, const u32x2& whi) {
  const f32x4 z = {0.f, 0.f, 0.f, 0.f};
  return mat4(whi, (u32x2){rb.z, rb.w}, mat4(wlo, (u32x2){rb.x, rb.y}, z));
}
__device__ __forceinline__ u32x2 wcol(const u16* __restrict__ w, int r0, int rs, int c) {
  return (u32x2){(u32)w[r0 * rs + c] | ((u32)w[(r0 + 1) * rs + c] << 16), (u32)w[(r0 + 2) * rs + c] | ((u32)w[(r0 + 3) * rs + c] << 16)};
}

__global__ void __launch_bounds__(NTH) k_acc(const u32x2* __restrict__ mrow, const u16* __restrict__ dist,
                                             const u32* __restrict__ centers, const u32* __restrict__ freq,
                                             const u16* __restrict__ w_rbf, const u16* __restrict__ w_sbf,
                                             const u32* __restrict__ w_down, const u16* __restrict__ w_up,
                                             const char* __restrict__ ws, u32x2* __restrict__ outp) {
  __shared__ __attribute__((aligned(16))) AccLds S;
  const int t = threadIdx.x;
  const int lane = t & 63;
  const int wave = __builtin_amdgcn_readfirstlane(t >> 6);
  const int b = (blockIdx.x & 7) * 32 + (blockIdx.x >> 3);
  const int c4 = lane & 3;
  const rsrc_t r_rec = make_rsrc(ws + WS_REC, (u32)NTILES * TSTRIDE * 12u);
  const rsrc_t r_m0 = make_rsrc(mrow, EH * 8u), r_m1 = make_rsrc(mrow + EH, EH * 8u);

  ST(32); RT(50); SP(0);
  __builtin_amdgcn_s_setprio(3);
  const u32 tile = lane * 16 + wave;
  const u32* tab = (const u32*)(ws + WS_TAB) + (size_t)tile * TABS;
  const u32 ent0 = tab[b], ent1 = tab[256 + b];
  const u32 mxw = ((const u32*)(ws + WS_MAX))[t & (NWG - 1)];
  u32 dv[4];
  {
    const rsrc_t r_dist = make_rsrc(dist + b * EB, EB * 2u);
#pragma unroll
    for (int j = 0; j < 4; j++) dv[j] = __builtin_amdgcn_raw_buffer_load_b16(r_dist, (j * NTH + t) * 2u, 0, 0);
  }
  const u32x2 ws0l = wcol(w_sbf, 0, 8, c4), ws0h = wcol(w_sbf, 4, 8, c4);
  const u32x2 ws1l = wcol(w_sbf, 0, 8, 4 + c4), ws1h = wcol(w_sbf, 4, 8, 4 + c4);
  const u32x2 wrl = wcol(w_rbf, 0, 4, c4), wrh = wcol(w_rbf, 4, 4, c4);

  Runs R = make_runs(ent0, tile);
  SP(1);
  Burst B;
  Pipe Q;
  u32 off0[NW];
  map_burst(off0, R, S.bmap[wave], lane);
  SP(2);
  load_burst<0, GDEPTH>(B, off0, r_rec);
  SP(3);

  if (t == 0) S.flag = 0;
  {
    const u32 mb = __builtin_amdgcn_readlane(wave_incl_max(mxw), 63);
    if (lane == 0) S.red[wave] = mb;
  }
  u32x2 gate[4];
  u32 kexp = 0;
  bool bad = false;
#pragma unroll
  for (int j = 0; j < 4; j++) {
    const int i = j * NTH + t;
    gate[j] = (u32x2){0, 0};
    if (j < 3 || wave == 0) {
      const u32x4 rb = edge_rbf(bf2f(dv[j]), centers);
      const f32x4 A0 = rb_times(rb, ws0l, ws0h), A1 = rb_times(rb, ws1l, ws1h), g = rb_times(rb, wrl, wrh);
      gate[j] = (u32x2){pack_bf16(g.x, g.y), pack_bf16(g.z, g.w)};
      const float n = fmaxf(fmaxf(fabsf(A0.x) + fabsf(A1.x), fabsf(A0.y) + fabsf(A1.y)),
                            fmaxf(fabsf(A0.z) + fabsf(A1.z), fabsf(A0.w) + fabsf(A1.w)));
      int e = __builtin_amdgcn_frexp_expf(n);
      e = e < -120 ? -120 : (e > 120 ? 120 : e);
      bad |= i < EB && !(n < 1e36f);
      const float sc = __builtin_amdgcn_ldexpf(1.f, -e);
      const u32x4 a = {pack_bf16(A0.x * sc, A1.x * sc), pack_bf16(A0.y * sc, A1.y * sc),
                       pack_bf16(A0.z * sc, A1.z * sc), pack_bf16(A0.w * sc, A1.w * sc)};
      kexp |= (u32)(e & 0xff) << (8 * j);
      if (i < EP) {
        S.at[i] = i < EB ? a : (u32x4){0, 0, 0, 0};
        S.a01[i] = 0; S.a23[i] = 0; S.cnt[i] = 0;
      }
    }
    __builtin_amdgcn_sched_barrier(0);
  }
  SP(4);
  const u32x2 wdc = wcol((const u16*)w_down, 0, 4, c4);
  __builtin_amdgcn_s_setprio(0);
  start_burst(Q, B, R.total, r_m0, lane);
  load_burst<GDEPTH, NW>(B, off0, r_rec);
  SP(5);
  ST(33);
  lds_barrier();
  if (bad) S.flag = 2;
  ST(34); RT(52); SP(6);

  u32 mall = umax(umax(S.red[0], S.red[1]), umax(S.red[2], S.red[3]));
  mall = __builtin_amdgcn_readfirstlane(mall);
  const float Bm = bf2f(mall);
  int ks[4];
  bool fixed_ok = true;
#pragma unroll
  for (int d = 0; d < 4; d++) {
    float cs = 0.f;
#pragma unroll
    for (int k = 0; k < 4; k++) cs += fabsf(wel(w_down, k * 4 + d));
    const float Bd = Bm * cs * 1.02f;
    const bool zero = (Bd == 0.f);
    if (!zero && !(Bd > 1e-25f && Bd < 1e25f)) fixed_ok = false;
    ks[d] = __builtin_amdgcn_readfirstlane(zero ? 0 : 22 - __builtin_amdgcn_frexp_expf(Bd));
  }
  Consts C;
  C.f0 = wel(freq, 0);
  C.f1 = wel(freq, 1);
  {
    const int kc = c4 == 0 ? ks[0] : (c4 == 1 ? ks[1] : (c4 == 2 ? ks[2] : ks[3]));
    C.wdT = (u32x2){pack_bf16(__builtin_amdgcn_ldexpf(bflo(wdc.x), kc), __builtin_amdgcn_ldexpf(bfhi(wdc.x), kc)),
                    pack_bf16(__builtin_amdgcn_ldexpf(bflo(wdc.y), kc), __builtin_amdgcn_ldexpf(bfhi(wdc.y), kc))};
  }

  bool use_float = !fixed_ok;
  if (!use_float) {
#pragma unroll 1
    for (int h = 0; h < 2; h++) {
      const rsrc_t r_m = h ? r_m1 : r_m0;
      run_burst(S, C, Q, B, R.total, r_m, lane);
      if (R.total > NW * 64) run_simple<0>(S, C, R, NW * 64, r_rec, r_m, lane);
      ST(35 + h);
      if (h == 0) {
        R = make_runs(ent1, tile);
        u32 off[NW];
        map_burst(off, R, S.bmap[wave], lane);
        load_burst<0, NW>(B, off, r_rec);
        lds_barrier();
        start_burst(Q, B, R.total, r_m1, lane);
      }
    }
    lds_barrier();
    ST(37); RT(53);
    u32 over = 0;
#pragma unroll
    for (int j = 0; j < 4; j++) over |= (j * NTH + t < EB) && S.cnt[j * NTH + t < EB ? j * NTH + t : 0] >= 512u;
    if (over) S.flag = 1;
    lds_barrier();
    use_float = __builtin_amdgcn_readfirstlane(S.flag) != 0;
    if (use_float) {
      for (int i = t; i < EP; i += NTH) { S.a01[i] = 0; S.a23[i] = 0; }
      lds_barrier();
    }
  }
  if (use_float) {
    C.wdT = wdc;
#pragma unroll
    for (int d = 0; d < 4; d++) ks[d] = 0;
#pragma unroll 1
    for (int h = 0; h < 2; h++)
      run_simple<1>(S, C, make_runs(h ? ent1 : ent0, tile), 0, r_rec, h ? r_m1 : r_m0, lane);
    lds_barrier();
  }

  ST(38);
  const rsrc_t r_mb = make_rsrc(mrow + b * EB, EB * 8u);
  const rsrc_t r_o = make_rsrc(outp + b * EB, EB * 8u);
  u32x2 mr[4];
#pragma unroll
  for (int j = 0; j < 4; j++) mr[j] = __builtin_amdgcn_raw_buffer_load_b64(r_mb, (j * NTH + t) * 8u, 0, 0);
  const u32x2 wuc = wcol(w_up, 0, 4, c4);
#pragma unroll
  for (int j = 0; j < 4; j++) {
    const int i = j * NTH + t;
    if (j < 3 || wave == 0) {
      const int ii = i < EB ? i : 0;
      float ag[4];
      const u64 w01 = S.a01[ii], w23 = S.a23[ii];
      if (!use_float) {
        const u64 off = (u64)S.cnt[ii] * (((u64)MAGIC_BITS << 32) + MAGIC_BITS);
        const u64 v01 = w01 - off, v23 = w23 - off;
        const int l0 = (int)(u32)v01, l2 = (int)(u32)v23;
        const int h1 = (int)(u32)((v01 - (u64)(long long)l0) >> 32), h3 = (int)(u32)((v23 - (u64)(long long)l2) >> 32);
        ag[0] = (float)l0; ag[1] = (float)h1; ag[2] = (float)l2; ag[3] = (float)h3;
      } else {
        ag[0] = __uint_as_float((u32)w01); ag[1] = __uint_as_float((u32)(w01 >> 32));
        ag[2] = __uint_as_float((u32)w23); ag[3] = __uint_as_float((u32)(w23 >> 32));
      }
      const int ke = (int)(signed char)(kexp >> (8 * j));
#pragma unroll
      for (int d = 0; d < 4; d++) ag[d] = __builtin_amdgcn_ldexpf(ag[d], ke - ks[d]);
      const f32x4 z = {0.f, 0.f, 0.f, 0.f};
      const f32x4 u = mat4(wuc, (u32x2){pack_bf16(ag[0], ag[1]), pack_bf16(ag[2], ag[3])}, z);
      const float o0 = bflo(mr[j].x) + u.x * bflo(gate[j].x), o1 = bfhi(mr[j].x) + u.y * bfhi(gate[j].x);
      const float o2 = bflo(mr[j].y) + u.z * bflo(gate[j].y), o3 = bfhi(mr[j].y) + u.w * bfhi(gate[j].y);
      __builtin_amdgcn_raw_buffer_store_b64((u32x2){pack_bf16(o0, o1), pack_bf16(o2, o3)}, r_o, i < EB ? i * 8u : OOB, 0, 0);
    }
    __builtin_amdgcn_sched_barrier(0);
  }
  ST(39); RT(51);
}

extern "C" void kernel_launch(void* const* d_in, const int* in_sizes, int n_in, void* d_out, int out_size,
                              void* d_ws, size_t ws_size, hipStream_t stream) {
  const u32x2* m = (const u32x2*)d_in[0];
  const u32* idx_in = (const u32*)d_in[1];
  const u32* idx_out = (const u32*)d_in[2];
  const u16* dist = (const u16*)d_in[3];
  const u16* angle = (const u16*)d_in[4];
  const u32* centers = (const u32*)d_in[5];
  const u32* freq = (const u32*)d_in[6];
  const u16* w_rbf = (const u16*)d_in[7];
  const u16* w_sbf = (const u16*)d_in[8];
  const u32* w_down = (const u32*)d_in[9];
  const u16* w_up = (const u16*)d_in[10];
  char* ws = (char*)d_ws + ((size_t)WS_OFF_MB << 20);
  k_part<<<NWG, NTH, 0, stream>>>(idx_in, idx_out, angle, m, ws);
  k_acc<<<NB, NTH, 0, stream>>>(m, dist, centers, freq, w_rbf, w_sbf, w_down, w_up, (const char*)ws, (u32x2*)d_out);
}
```

```cpp
#include <hip/hip_runtime.h>
#include <hip/hip_bf16.h>
#include <stdint.h>

typedef unsigned int u32;
typedef unsigned long long u64;
typedef unsigned short u16;
typedef u32 u32x2 __attribute__((ext_vector_type(2)));
typedef u32 u32x3 __attribute__((ext_vector_type(3)));
typedef u32 u32x4 __attribute__((ext_vector_type(4)));
typedef float f32x4 __attribute__((ext_vector_type(4)));
typedef short s16x4 __attribute__((ext_vector_type(4)));
typedef __bf16 bf16x2_t __attribute__((ext_vector_type(2)));
typedef float f32x2_t __attribute__((ext_vector_type(2)));

#define GDEPTH 2
#ifndef M_AUX
#define M_AUX 2
#endif
#define POLL_SLEEP 8

constexpr int E_ = 800000;
constexpr int T_ = 12800000;
constexpr int EB = 3125;
constexpr int EH = E_ / 2;
constexpr int NWG = 256;
constexpr int NTH = 1024;
constexpr int NT = 4;
constexpr int TILE = 12500;
constexpr int TILE_MAX = 12544;
constexpr int NTILES = NWG * NT;
constexpr int NSLOT = TILE_MAX + 512;
constexpr int TSTRIDE = 6528;
constexpr int TABS = 544;
constexpr u32 NULLREC = (u32)EB;
constexpr int EP = EB + 3;
constexpr int NW = 14;

constexpr size_t WS_EPOCH = 0;
constexpr size_t WS_FLAG = 128;
constexpr size_t WS_MAX = WS_FLAG + 2048;
constexpr size_t WS_TAB = 8192;
constexpr size_t WS_REC = WS_TAB + (size_t)NTILES * TABS * 4;
static_assert(WS_MAX + 2048 <= WS_TAB && WS_REC % 128 == 0 && (TABS * 4) % 128 == 0, "layout");
static_assert(NSLOT / 2 <= TSTRIDE, "tile region");
static_assert(WS_REC + (size_t)NTILES * TSTRIDE * 12 <= (size_t)256 * 1024 * 1024, "ws");

__device__ __forceinline__ float bf2f(u32 h) { return __uint_as_float(h << 16); }
__device__ __forceinline__ float bflo(u32 w) { return __uint_as_float(w << 16); }
__device__ __forceinline__ float bfhi(u32 w) { return __uint_as_float(w & 0xffff0000u); }
__device__ __forceinline__ u32 pack_bf16(float a, float b) {
  f32x2_t v = {a, b};
  bf16x2_t r = __builtin_convertvector(v, bf16x2_t);
  return __builtin_bit_cast(u32, r);
}
__device__ __forceinline__ int fresh(int x) { asm volatile("" : "+v"(x)); return x; }

__device__ __forceinline__ void lds_barrier() {
  asm volatile("s_waitcnt lgkmcnt(0)\n\ts_barrier" ::: "memory");
}

template <int CTRL, int RM>
__device__ __forceinline__ u32 dpp0(u32 v) { return (u32)__builtin_amdgcn_update_dpp(0, (int)v, CTRL, RM, 0xf, false); }
__device__ __forceinline__ u32 wave_incl_add(u32 v) {
  v += dpp0<0x111, 0xf>(v);
  v += dpp0<0x112, 0xf>(v);
  v += dpp0<0x114, 0xf>(v);
  v += dpp0<0x118, 0xf>(v);
  v += dpp0<0x142, 0xa>(v);
  v += dpp0<0x143, 0xc>(v);
  return v;
}
__device__ __forceinline__ u32 umax(u32 a, u32 b) { return a > b ? a : b; }
__device__ __forceinline__ u32 wave_incl_max(u32 v) {
  v = umax(v, dpp0<0x111, 0xf>(v));
  v = umax(v, dpp0<0x112, 0xf>(v));
  v = umax(v, dpp0<0x114, 0xf>(v));
  v = umax(v, dpp0<0x118, 0xf>(v));
  v = umax(v, dpp0<0x142, 0xa>(v));
  v = umax(v, dpp0<0x143, 0xc>(v));
  return v;
}

typedef __amdgpu_buffer_rsrc_t rsrc_t;
__device__ __forceinline__ rsrc_t make_rsrc(const void* p, u32 bytes) {
  return __builtin_amdgcn_make_buffer_rsrc((void*)p, 0, bytes, 0x00020000);
}
constexpr u32 OOB = 0x80000000u;
constexpr int AUX_NT = 2;
constexpr int AUX_DEV = 16;

__device__ __forceinline__ u64 load_dev(const u64* p) { return __hip_atomic_load(p, __ATOMIC_RELAXED, __HIP_MEMORY_SCOPE_AGENT); }
__device__ __forceinline__ void store_dev(u64* p, u64 v) { __hip_atomic_store(p, v, __ATOMIC_RELAXED, __HIP_MEMORY_SCOPE_AGENT); }

#define RT(i)

__device__ __forceinline__ f32x4 mat4(const u32x2& wT, const u32x2& row, const f32x4& acc) {
  return __builtin_amdgcn_mfma_f32_4x4x4bf16_1k(__builtin_bit_cast(s16x4, wT), __builtin_bit_cast(s16x4, row), acc, 0, 0, 0);
}
__device__ __forceinline__ float wel(const u32* __restrict__ w, int i) { return (i & 1) ? bfhi(w[i >> 1]) : bflo(w[i >> 1]); }

__device__ __forceinline__ u32x4 edge_rbf(float d, const u32* __restrict__ centers) {
  u32x4 rb;
#pragma unroll
  for (int r = 0; r < 4; r++) {
    u32 p = pack_bf16(d - wel(centers, 2 * r), d - wel(centers, 2 * r + 1));
    float x = bflo(p), y = bfhi(p);
    p = pack_bf16(x * x, y * y);
    p = pack_bf16(-10.0f * bflo(p), -10.0f * bfhi(p));
    rb[r] = pack_bf16(__expf(bflo(p)), __expf(bfhi(p)));
  }
  return rb;
}
__device__ __forceinline__ f32x4 rb_times(const u32x4& rb, const u32x2& wlo, const u32x2& whi) {
  const f32x4 z = {0.f, 0.f, 0.f, 0.f};
  return mat4(whi, (u32x2){rb.z, rb.w}, mat4(wlo, (u32x2){rb.x, rb.y}, z));
}

struct PartLds {
  u64 stage[NSLOT + 4];
  u32 hist[512];
  u32 cursor[512];
  u32 sinfo[4];
  u32 smax[16];
};
struct AccLds {
  u64 a01[EP];
  u64 a23[EP];
  u32x4 at[EP];
  u32 cnt[EP];
  u64 bmap[16][16];
  u32 flag;
};
struct Lds {
  union { PartLds part; AccLds acc; };
  u16 dist[4 * NTH];
  u32x2 wcols[4][8];
  u16 w[128];
};
static_assert(sizeof(Lds) <= 160 * 1024, "LDS");

struct Raw { u32x4 in, out; u32x2 an; };

constexpr float MAGIC = 12582912.0f;
constexpr u32 MAGIC_BITS = 0x4B400000u;

struct Consts { u32x2 wdT; float f0, f1; const u32x4* at; };

__device__ __forceinline__ float dot2bf(u32 a, u32 b) {
  return __builtin_amdgcn_fdot2_f32_bf16(__builtin_bit_cast(bf16x2_t, a), __builtin_bit_cast(bf16x2_t, b), 0.f, false);
}

template <int MODE>
__device__ __forceinline__ void add_record(AccLds& S, const Consts& C, const f32x4& md, u32 loc, float ang) {
  const u32 pk = pack_bf16(C.f0 * ang, C.f1 * ang);
  const float INV2PI = 0.15915494309189535f;
  const float c0 = __builtin_amdgcn_cosf(__builtin_amdgcn_fractf(bflo(pk) * INV2PI));
  const float c1 = __builtin_amdgcn_cosf(__builtin_amdgcn_fractf(bfhi(pk) * INV2PI));
  const u32 cp = pack_bf16(c0, c1);
  const u32x4 a = C.at[loc];
  const float s0 = dot2bf(a.x, cp), s1 = dot2bf(a.y, cp), s2 = dot2bf(a.z, cp), s3 = dot2bf(a.w, cp);
  if (MODE == 0) {
    const u32 t0 = __float_as_uint(__builtin_fmaf(md.x, s0, MAGIC)), t1 = __float_as_uint(__builtin_fmaf(md.y, s1, MAGIC));
    const u32 t2 = __float_as_uint(__builtin_fmaf(md.z, s2, MAGIC)), t3 = __float_as_uint(__builtin_fmaf(md.w, s3, MAGIC));
    atomicAdd(&S.a01[loc], (u64)t0 | ((u64)t1 << 32));
    atomicAdd(&S.a23[loc], (u64)t2 | ((u64)t3 << 32));
    atomicAdd(&S.cnt[loc], 1u);
  } else {
    float* p01 = (float*)&S.a01[loc];
    float* p23 = (float*)&S.a23[loc];
    atomicAdd(p01, md.x * s0);
    atomicAdd(p01 + 1, md.y * s1);
    atomicAdd(p23, md.z * s2);
    atomicAdd(p23 + 1, md.w * s3);
  }
}

struct Runs { u32 cnt, P, base, total; };
__device__ __forceinline__ Runs make_runs(u32 ent, u32 tile) {
  Runs R;
  R.cnt = ent >> 16;
  const u32 incl = wave_incl_add(R.cnt);
  R.P = incl - R.cnt;
  R.total = __builtin_amdgcn_readlane(incl, 63);
  R.base = tile * TSTRIDE + (ent & 0xffffu) - R.P;
  return R;
}

struct Burst { u32x3 u[NW]; };
__device__ __forceinline__ void map_burst(u32 (&off)[NW], const Runs& R, u64* bm, int lane) {
  const bool ne = R.cnt != 0;
  const u64 nz = __ballot(ne);
  const u32 rank = __builtin_amdgcn_mbcnt_hi((u32)(nz >> 32), __builtin_amdgcn_mbcnt_lo((u32)nz, 0));
  const u32 cbase = (u32)__builtin_amdgcn_ds_permute((int)(ne ? rank * 4 : 252u), (int)R.base);
  if (lane < 16) bm[lane] = 0;
  if (ne && R.P < NW * 64) atomicOr((u32*)bm + (R.P >> 5), 1u << (R.P & 31));
  const u64 word = bm[lane & 15];
  const u32 wlo = (u32)word, whi = (u32)(word >> 32);
  u32 cum = 0;
#pragma unroll
  for (int j = 0; j < NW; j++) {
    const u64 M = (u64)(u32)__builtin_amdgcn_readlane(wlo, j) | ((u64)(u32)__builtin_amdgcn_readlane(whi, j) << 32);
    const u32 r0 = cum + (u32)(M & 1) - 1;
    const u64 M1 = M >> 1;
    const u32 r = __builtin_amdgcn_mbcnt_hi((u32)(M1 >> 32), __builtin_amdgcn_mbcnt_lo((u32)M1, r0));
    cum += __builtin_popcountll(M);
    const u32 u = j * 64 + lane;
    const u32 v = (u32)__builtin_amdgcn_ds_bpermute((int)(r * 4), (int)cbase);
    off[j] = u < R.total ? __umul24(v + u, 12u) : OOB;
  }
}
__device__ __forceinline__ void load_burst(Burst& B, const u32 (&off)[NW], rsrc_t r_rec, int j0, int j1) {
  __builtin_amdgcn_sched_barrier(0);
#pragma unroll
  for (int j = 0; j < NW; j++)
    if (j >= j0 && j < j1) B.u[j] = __builtin_amdgcn_raw_buffer_load_b96(r_rec, off[j], 0, AUX_DEV);
  __builtin_amdgcn_sched_barrier(0);
}

struct Win { u32x2 g0, g1; };
__device__ __forceinline__ void issue_win(Win& W, const u32x3& c, rsrc_t r_m, u32 u0, u32 total, int lane) {
  const bool v = u0 + lane < total;
  W.g0 = __builtin_amdgcn_raw_buffer_load_b64(r_m, v ? (c.x >> 9) & ~7u : OOB, 0, 0);
  W.g1 = __builtin_amdgcn_raw_buffer_load_b64(r_m, v ? (c.z >> 9) & ~7u : OOB, 0, 0);
}
template <int MODE>
__device__ __forceinline__ void proc_win(AccLds& S, const Consts& C, const Win& W, const u32x3& c, u32 u0, u32 total, int lane) {
  const f32x4 z = {0.f, 0.f, 0.f, 0.f};
  const f32x4 md0 = mat4(C.wdT, W.g0, z), md1 = mat4(C.wdT, W.g1, z);
  if (u0 + lane < total) {
    add_record<MODE>(S, C, md0, c.x & 0xfffu, bflo(c.y));
    add_record<MODE>(S, C, md1, c.z & 0xfffu, bfhi(c.y));
  }
}

struct Pipe { Win W[GDEPTH + 1]; };
__device__ __forceinline__ void start_burst(Pipe& Q, const Burst& B, u32 total, rsrc_t r_m, int lane) {
#pragma unroll
  for (int j = 0; j < GDEPTH; j++) issue_win(Q.W[j], B.u[j], r_m, j * 64, total, lane);
}
__device__ __forceinline__ void run_burst(AccLds& S, const Consts& C, Pipe& Q, const Burst& B, u32 total, rsrc_t r_m, int lane) {
#pragma unroll
  for (int j = 0; j < NW; j++) {
    if (j + GDEPTH < NW) issue_win(Q.W[(j + GDEPTH) % (GDEPTH + 1)], B.u[j + GDEPTH], r_m, (j + GDEPTH) * 64, total, lane);
    proc_win<0>(S, C, Q.W[j % (GDEPTH + 1)], B.u[j], j * 64, total, lane);
  }
}

template <int MODE>
__device__ __forceinline__ void run_simple(AccLds& S, const Consts& C, const Runs& R, u32 u0, rsrc_t r_rec, rsrc_t r_m, int lane) {
#pragma unroll 1
  for (; u0 < R.total; u0 += 64) {
    const u32 d = R.P - u0;
    const bool part = R.cnt != 0 && d - 1u < 63u;
    u32 v = (u32)__builtin_amdgcn_ds_permute((int)(part ? d * 4 : 0u), (int)R.base);
    const u64 m0 = __ballot(R.cnt != 0 && R.P <= u0);
    const u32 b0 = __builtin_amdgcn_readlane(R.base, 63 - __builtin_clzll(m0 | 1ull));
    v = wave_incl_max(lane == 0 ? b0 : v);
    const u32 u = u0 + lane;
    const u32x3 c = __builtin_amdgcn_raw_buffer_load_b96(r_rec, u < R.total ? __umul24(v + u, 12u) : OOB, 0, AUX_DEV);
    Win W;
    issue_win(W, c, r_m, u0, R.total, lane);
    proc_win<MODE>(S, C, W, c, u0, R.total, lane);
  }
}

__device__ __forceinline__ bool timed_out(u64 t0) { return __builtin_amdgcn_s_memrealtime() - t0 > 5000000ull; }

__global__ void __launch_bounds__(NTH) k_gemnet(const u32x2* __restrict__ mrow, const u32* __restrict__ idx_in,
                                                const u32* __restrict__ idx_out, const u16* __restrict__ dist,
                                                const u16* __restrict__ angle, const u32* __restrict__ centers,
                                                const u32* __restrict__ freq, const u16* __restrict__ w_rbf,
                                                const u16* __restrict__ w_sbf, const u32* __restrict__ w_down,
                                                const u16* __restrict__ w_up, char* __restrict__ ws,
                                                u32x2* __restrict__ outp) {
  __shared__ __attribute__((aligned(16))) Lds lds;
  const int wave = __builtin_amdgcn_readfirstlane(threadIdx.x >> 6);
  const int w = blockIdx.x;
  const int b = (w & 7) * 32 + (w >> 3);
  u64 tag;

  {
    u64* const stage = lds.part.stage;
    u32* const hist = lds.part.hist;
    u32* const cursor = lds.part.cursor;
    const int t = threadIdx.x;
    const int lane = t & 63;
    const rsrc_t r_in = make_rsrc(idx_in, T_ * 4u), r_out = make_rsrc(idx_out, T_ * 4u), r_an = make_rsrc(angle, T_ * 2u);
    const rsrc_t r_dist = make_rsrc(dist + b * EB, EB * 2u);

    Raw raw[3];
    u32 xin, xout, xan;
    auto tile_start = [](u32 tile) { return (tile * TILE) & ~63u; };
    auto load_slot = [&](int k, int s, int t) {
      const u32 r0 = tile_start(w * NT + k);
      const u32 r = r0 + s * NTH * 4;
      const bool live = k < NT;
      __builtin_amdgcn_sched_barrier(0);
      if (s < 3) {
        const u32 vo16 = live ? t * 16u : OOB, vo8 = live ? t * 8u : OOB;
        raw[s].in = __builtin_amdgcn_raw_buffer_load_b128(r_in, vo16, r * 4u, AUX_NT);
        raw[s].out = __builtin_amdgcn_raw_buffer_load_b128(r_out, vo16, r * 4u, AUX_NT);
        raw[s].an = __builtin_amdgcn_raw_buffer_load_b64(r_an, vo8, r * 2u, AUX_NT);
      } else {
        const u32 nx = tile_start(w * NT + k + 1) - r0 - 12 * NTH;
        const u32 xo4 = (live && t < nx) ? t * 4u : OOB, xo2 = (live && t < nx) ? t * 2u : OOB;
        xin = __builtin_amdgcn_raw_buffer_load_b32(r_in, xo4, r * 4u, AUX_NT);
        xout = __builtin_amdgcn_raw_buffer_load_b32(r_out, xo4, r * 4u, AUX_NT);
        xan = __builtin_amdgcn_raw_buffer_load_b16(r_an, xo2, r * 2u, AUX_NT);
      }
      __builtin_amdgcn_sched_barrier(0);
    };
    u32 lo[13], hi[13];
    auto key = [&](u32 in, u32 out, u32 an16, u32& l, u32& h) {
      u32 q;
      asm("v_mul_hi_u32_u24 %0, %1, %2" : "=v"(q) : "v"(out), "v"(1374390u));
      const u32 loc = out - q * EB;
      const bool up = in >= (u32)EH;
      const u32 b4 = q * 4 + (up ? 1024u : 0u);
      l = ((in - (up ? (u32)EH : 0u)) << 12) | loc;
      h = an16 | (b4 << 16);
      return b4;
    };

    RT(48);
    {
      const u64 epoch = load_dev((const u64*)(ws + WS_EPOCH));
      u32 dv[4];
#pragma unroll
      for (int j = 0; j < 4; j++) dv[j] = __builtin_amdgcn_raw_buffer_load_b16(r_dist, (j * NTH + t) * 2u, 0, 0);
      const u16* wp = t < 64 ? w_sbf + t : (t < 96 ? w_rbf + (t - 64) : (t < 112 ? (const u16*)w_down + (t - 96) : w_up + (t - 112)));
      const u32 wv = t < 128 ? *wp : 0;
      const rsrc_t r_m = make_rsrc(mrow + w * EB, EB * 8u);
      u32x2 r[4];
#pragma unroll
      for (int i = 0; i < 4; i++) r[i] = __builtin_amdgcn_raw_buffer_load_b64(r_m, t * 8u, i * NTH * 8u, M_AUX);
#pragma unroll
      for (int s = 0; s < 4; s++) load_slot(0, s, t);
      if (t < 512) hist[t] = 0;
      u32 mx = 0;
#pragma unroll
      for (int i = 0; i < 4; i++) {
        const u32 keep = (i * NTH + t < EB) ? 0x7fffu : 0u;
        mx = umax(mx, umax(umax(r[i].x & keep, (r[i].x >> 16) & keep), umax(r[i].y & keep, (r[i].y >> 16) & keep)));
      }
      mx = wave_incl_max(mx);
      if (lane == 63) lds.part.smax[wave] = mx;
      if (t < 128) lds.w[t] = (u16)wv;
#pragma unroll
      for (int j = 0; j < 4; j++) lds.dist[j * NTH + t] = (u16)dv[j];
      const u64 e = epoch + 1;
      tag = (u64)(u32)__builtin_amdgcn_readfirstlane((u32)e) | ((u64)(u32)__builtin_amdgcn_readfirstlane((u32)(e >> 32)) << 32);
    }
    lds_barrier();
    if (t == 0) {
      u32 mx = 0;
#pragma unroll
      for (int i = 0; i < 16; i++) mx = umax(mx, lds.part.smax[i]);
      store_dev((u64*)(ws + WS_MAX) + w, (tag << 16) | mx);
    }
    if (t < 32) {
      const int c = t & 3, i = t >> 2;
      const int o = c + (i < 4 ? (i & 1) * 32 + (i >> 1) * 4 : 64 + (i - 4) * 16), st = i < 4 ? 8 : 4;
      lds.wcols[c][i] = (u32x2){(u32)lds.w[o] | ((u32)lds.w[o + st] << 16), (u32)lds.w[o + 2 * st] | ((u32)lds.w[o + 3 * st] << 16)};
    }
    RT(1);

#pragma unroll
    for (int u = 0; u < 7; u++) __builtin_amdgcn_raw_buffer_store_b96((u32x3){0, 0, 0}, make_rsrc(ws, 0), OOB + u * 16u, 0, 0);

#pragma unroll 1
    for (int k = 0; k < NT; k++) {
      const int tile = w * NT + k;
      const int t = fresh(threadIdx.x);
      const int lane = t & 63;
      const bool hasx = (u32)t < tile_start(tile + 1) - tile_start(tile) - 12 * NTH;
#pragma unroll
      for (int s = 0; s < 3; s++) {
#pragma unroll
        for (int j = 0; j < 4; j++) {
          const u32 aw = raw[s].an[j >> 1];
          const u32 b4 = key(raw[s].in[j], raw[s].out[j], (j & 1) ? (aw >> 16) : (aw & 0xffffu), lo[s * 4 + j], hi[s * 4 + j]);
          atomicAdd((u32*)((char*)hist + b4), 8u);
        }
      }
      {
        const u32 b4 = key(xin, xout, xan, lo[12], hi[12]);
        if (hasx) atomicAdd((u32*)((char*)hist + b4), 8u);
      }
      load_slot(k + 1, 0, t);
      lds_barrier();
      u32x4 c0, c1;
      u32 run;
      if (wave == 1) {
        c0 = *(const u32x4*)&hist[lane * 8];
        c1 = *(const u32x4*)&hist[lane * 8 + 4];
        u32x4 p0 = (c0 + 8u) & ~15u, p1 = (c1 + 8u) & ~15u;
        const u32 tot = p0.x + p0.y + p0.z + p0.w + p1.x + p1.y + p1.z + p1.w;
        const u32 incl = wave_incl_add(tot);
        run = incl - tot;
        u32x4 a, bb;
        a.x = run; a.y = a.x + p0.x; a.z = a.y + p0.y; a.w = a.z + p0.z;
        bb.x = a.w + p0.w; bb.y = bb.x + p1.x; bb.z = bb.y + p1.y; bb.w = bb.z + p1.z;
        *(u32x4*)&cursor[lane * 8] = a;
        *(u32x4*)&cursor[lane * 8 + 4] = bb;
        if (lane == 63) lds.part.sinfo[0] = incl >> 4;
      }
      lds_barrier();
      if (wave == 1) {
        *(u32x4*)&hist[lane * 8] = (u32x4){0, 0, 0, 0};
        *(u32x4*)&hist[lane * 8 + 4] = (u32x4){0, 0, 0, 0};
        u32 ent[8];
#pragma unroll
        for (int j = 0; j < 8; j++) {
          const u32 c = j < 4 ? c0[j & 3] : c1[j & 3];
          const u32 p = (c + 8u) & ~15u;
          ent[j] = (run >> 4) | (p << 12);
          *(u64*)((char*)stage + ((c & 8u) ? run + c : (u32)(NSLOT * 8))) = (u64)NULLREC;
          run += p;
        }
        u32x4 e0, e1;
#pragma unroll
        for (int j = 0; j < 4; j++) {
          const auto sw = __builtin_amdgcn_permlane32_swap(ent[j], ent[j + 4], false, false);
          if (j < 2) { e0[2 * j] = sw[0]; e0[2 * j + 1] = sw[1]; } else { e1[2 * j - 4] = sw[0]; e1[2 * j - 3] = sw[1]; }
        }
        const rsrc_t r_tab = make_rsrc(ws + WS_TAB + (size_t)tile * (TABS * 4), 2048);
        const u32 to = (lane & 31) * 64u + (lane >> 5) * 32u;
        __builtin_amdgcn_raw_buffer_store_b128(e0, r_tab, to, 0, AUX_DEV);
        __builtin_amdgcn_raw_buffer_store_b128(e1, r_tab, to + 16u, 0, AUX_DEV);
      }
      {
        u32 pos[13];
#pragma unroll
        for (int j = 0; j < 12; j++) pos[j] = atomicAdd((u32*)((char*)cursor + (hi[j] >> 16)), 8u);
        if (hasx) pos[12] = atomicAdd((u32*)((char*)cursor + (hi[12] >> 16)), 8u);
        load_slot(k + 1, 1, t);
#pragma unroll
        for (int j = 0; j < 12; j++) *(u64*)((char*)stage + pos[j]) = (u64)lo[j] | ((u64)hi[j] << 32);
        if (hasx) *(u64*)((char*)stage + pos[12]) = (u64)lo[12] | ((u64)hi[12] << 32);
      }
      load_slot(k + 1, 2, t);
      load_slot(k + 1, 3, t);
      lds_barrier();
      {
        const u32 nu = __builtin_amdgcn_readfirstlane(lds.part.sinfo[0]);
        const rsrc_t r_rec = make_rsrc(ws + WS_REC + (size_t)tile * (TSTRIDE * 12), TSTRIDE * 12);
#pragma unroll
        for (int u = 0; u < 7; u++) {
          const u32 j = u * NTH + t;
          const u32x4 q = *(const u32x4*)&stage[j < nu ? 2 * j : 0];
          const u32x3 v = {q.x, __builtin_amdgcn_perm(q.w, q.y, 0x05040100u), q.z};
          __builtin_amdgcn_raw_buffer_store_b96(v, r_rec, j < nu ? j * 12u : OOB, 0, AUX_DEV);
        }
      }
      RT(2 + k);
    }
  }

  asm volatile("s_waitcnt vmcnt(0)" ::: "memory");
  lds_barrier();
  RT(49);
  AccLds& S = lds.acc;
  const int t = fresh(threadIdx.x);
  const int lane = t & 63;
  const int c4 = lane & 3;
  if (t == 0) store_dev((u64*)(ws + WS_FLAG) + (w & 15) * 16 + (w >> 4), tag);
  if (t == 0) S.flag = 0;
#pragma unroll
  for (int j = 0; j < 4; j++) {
    const int i = j * NTH + t;
    if (i < EP) { S.a01[i] = 0; S.a23[i] = 0; S.cnt[i] = 0; }
  }
  lds_barrier();

  const rsrc_t r_rec = make_rsrc(ws + WS_REC, (u32)NTILES * TSTRIDE * 12u);
  const rsrc_t r_m0 = make_rsrc(mrow, EH * 8u), r_m1 = make_rsrc(mrow + EH, EH * 8u);
  const u32 tile = ((lane >> 2) * 16 + wave) * 4 + (lane & 3);
  const u64* pmax = (const u64*)(ws + WS_MAX) + lane;
  const u64* pflag = (const u64*)(ws + WS_FLAG) + wave * 16 + (lane & 15);
  const u64 t0 = __builtin_amdgcn_s_memrealtime();
  u64 g0 = load_dev(pmax), g1 = load_dev(pmax + 64), g2 = load_dev(pmax + 128), g3 = load_dev(pmax + 192);
  const bool ready = __all(load_dev(pflag) == tag);
  u32 mall, ent0, ent1;
  Runs R;
  Burst B;
  Pipe Q;
  u32x2 gate[4];
  u32 kexp = 0;
  bool bad = false;
  auto edge_tables = [&]() {
    const u32x4* wc = (const u32x4*)lds.wcols[c4];
    const u32x4 w0 = wc[0], w1 = wc[1], w2 = wc[2];
#pragma unroll
    for (int j = 0; j < 4; j++) {
      const int i = j * NTH + t;
      gate[j] = (u32x2){0, 0};
      if (j < 3 || wave == 0) {
        const u32x4 rb = edge_rbf(bf2f(lds.dist[i]), centers);
        const f32x4 A0 = rb_times(rb, (u32x2){w0.x, w0.y}, (u32x2){w0.z, w0.w});
        const f32x4 A1 = rb_times(rb, (u32x2){w1.x, w1.y}, (u32x2){w1.z, w1.w});
        const f32x4 g = rb_times(rb, (u32x2){w2.x, w2.y}, (u32x2){w2.z, w2.w});
        gate[j] = (u32x2){pack_bf16(g.x, g.y), pack_bf16(g.z, g.w)};
        const float n = fmaxf(fmaxf(fabsf(A0.x) + fabsf(A1.x), fabsf(A0.y) + fabsf(A1.y)),
                              fmaxf(fabsf(A0.z) + fabsf(A1.z), fabsf(A0.w) + fabsf(A1.w)));
        int e = __builtin_amdgcn_frexp_expf(n);
        e = e < -120 ? -120 : (e > 120 ? 120 : e);
        bad |= i < EB && !(n < 1e36f);
        const float sc = __builtin_amdgcn_ldexpf(1.f, -e);
        const u32x4 a = {pack_bf16(A0.x * sc, A1.x * sc), pack_bf16(A0.y * sc, A1.y * sc),
                         pack_bf16(A0.z * sc, A1.z * sc), pack_bf16(A0.w * sc, A1.w * sc)};
        kexp |= (u32)(e & 0xff) << (8 * j);
        if (i < EP) S.at[i] = i < EB ? a : (u32x4){0, 0, 0, 0};
      }
      __builtin_amdgcn_sched_barrier(0);
    }
  };
  if (!ready) edge_tables();
  {
    const u64 want = (tag << 16) >> 16;
    if (!ready)
      while (!__all(load_dev(pflag) == tag) && !timed_out(t0)) __builtin_amdgcn_s_sleep(POLL_SLEEP);
    while (!__all((g0 >> 16) == want && (g1 >> 16) == want && (g2 >> 16) == want && (g3 >> 16) == want) && !timed_out(t0)) {
      __builtin_amdgcn_s_sleep(POLL_SLEEP);
      g0 = load_dev(pmax); g1 = load_dev(pmax + 64); g2 = load_dev(pmax + 128); g3 = load_dev(pmax + 192);
    }
    __builtin_amdgcn_fence(__ATOMIC_ACQUIRE, "wavefront");
    RT(52);
    const u32x2 ent01 = __builtin_amdgcn_raw_buffer_load_b64(make_rsrc(ws + WS_TAB, NTILES * TABS * 4), tile * (TABS * 4) + b * 8, 0, AUX_DEV);
    const u32 mx = umax(umax((u32)g0 & 0xffffu, (u32)g1 & 0xffffu), umax((u32)g2 & 0xffffu, (u32)g3 & 0xffffu));
    mall = __builtin_amdgcn_readlane(wave_incl_max(mx), 63);
    if (w == 0 && t == 0) store_dev((u64*)(ws + WS_EPOCH), tag);
    ent0 = ent01.x; ent1 = ent01.y;
    R = make_runs(ent0, tile);
    u32 off0[NW];
    map_burst(off0, R, S.bmap[wave], lane);
    load_burst(B, off0, r_rec, 0, GDEPTH);
    start_burst(Q, B, R.total, r_m0, lane);
    load_burst(B, off0, r_rec, GDEPTH, NW);
  }
  if (ready) edge_tables();
  lds_barrier();
  if (bad) S.flag = 2;

  const float Bm = bf2f(mall);
  int ks[4];
  bool fixed_ok = true;
#pragma unroll
  for (int d = 0; d < 4; d++) {
    float cs = 0.f;
#pragma unroll
    for (int k = 0; k < 4; k++) cs += fabsf(wel(w_down, k * 4 + d));
    const float Bd = Bm * cs * 1.02f;
    const bool zero = (Bd == 0.f);
    if (!zero && !(Bd > 1e-25f && Bd < 1e25f)) fixed_ok = false;
    ks[d] = __builtin_amdgcn_readfirstlane(zero ? 0 : 22 - __builtin_amdgcn_frexp_expf(Bd));
  }
  const u32x2 wdc = lds.wcols[c4][6];
  Consts C;
  C.at = S.at;
  C.f0 = wel(freq, 0);
  C.f1 = wel(freq, 1);
  {
    const int kc = c4 == 0 ? ks[0] : (c4 == 1 ? ks[1] : (c4 == 2 ? ks[2] : ks[3]));
    C.wdT = (u32x2){pack_bf16(__builtin_amdgcn_ldexpf(bflo(wdc.x), kc), __builtin_amdgcn_ldexpf(bfhi(wdc.x), kc)),
                    pack_bf16(__builtin_amdgcn_ldexpf(bflo(wdc.y), kc), __builtin_amdgcn_ldexpf(bfhi(wdc.y), kc))};
  }

  const int te = fresh(threadIdx.x);
  u32x2 mr[4];
  auto load_rows = [&]() {
    const rsrc_t r_mb = make_rsrc(mrow + b * EB, EB * 8u);
#pragma unroll
    for (int j = 0; j < 4; j++) mr[j] = __builtin_amdgcn_raw_buffer_load_b64(r_mb, (j * NTH + te) * 8u, 0, 0);
  };
  bool use_float = !fixed_ok;
  if (!use_float) {
#pragma unroll 1
    for (int h = 0; h < 2; h++) {
      const rsrc_t r_m = h ? r_m1 : r_m0;
      run_burst(S, C, Q, B, R.total, r_m, lane);
      if (R.total > NW * 64) run_simple<0>(S, C, R, NW * 64, r_rec, r_m, lane);
      if (h == 0) {
        R = make_runs(ent1, tile);
        u32 off[NW];
        map_burst(off, R, S.bmap[wave], lane);
        load_burst(B, off, r_rec, 0, NW);
        lds_barrier();
        RT(62);
        start_burst(Q, B, R.total, r_m1, lane);
      }
    }
    load_rows();
    lds_barrier();
    RT(53);
    u32 over = 0;
#pragma unroll
    for (int j = 0; j < 4; j++) over |= (j * NTH + t < EB) && S.cnt[j * NTH + t < EB ? j * NTH + t : 0] >= 512u;
    if (over) S.flag = 1;
    lds_barrier();
    use_float = __builtin_amdgcn_readfirstlane(S.flag) != 0;
    if (use_float) {
      for (int i = t; i < EP; i += NTH) { S.a01[i] = 0; S.a23[i] = 0; }
      lds_barrier();
    }
  } else {
    __builtin_amdgcn_s_waitcnt(0x0F70);
    load_rows();
  }
  if (use_float) {
    C.wdT = wdc;
#pragma unroll
    for (int d = 0; d < 4; d++) ks[d] = 0;
#pragma unroll 1
    for (int h = 0; h < 2; h++)
      run_simple<1>(S, C, make_runs(h ? ent1 : ent0, tile), 0, r_rec, h ? r_m1 : r_m0, lane);
    lds_barrier();
  }

  const rsrc_t r_o = make_rsrc(outp + b * EB, EB * 8u);
  const u32x2 wuc = lds.wcols[c4][7];
#pragma unroll
  for (int j = 0; j < 4; j++) {
    const int i = j * NTH + te;
    if (j < 3 || wave == 0) {
      const int ii = i < EB ? i : 0;
      float ag[4];
      const u64 w01 = S.a01[ii], w23 = S.a23[ii];
      if (!use_float) {
        const u64 off = (u64)S.cnt[ii] * (((u64)MAGIC_BITS << 32) + MAGIC_BITS);
        const u64 v01 = w01 - off, v23 = w23 - off;
        const int l0 = (int)(u32)v01, l2 = (int)(u32)v23;
        const int h1 = (int)(u32)((v01 - (u64)(long long)l0) >> 32), h3 = (int)(u32)((v23 - (u64)(long long)l2) >> 32);
        ag[0] = (float)l0; ag[1] = (float)h1; ag[2] = (float)l2; ag[3] = (float)h3;
      } else {
        ag[0] = __uint_as_float((u32)w01); ag[1] = __uint_as_float((u32)(w01 >> 32));
        ag[2] = __uint_as_float((u32)w23); ag[3] = __uint_as_float((u32)(w23 >> 32));
      }
      const int ke = (int)(signed char)(kexp >> (8 * j));
#pragma unroll
      for (int d = 0; d < 4; d++) ag[d] = __builtin_amdgcn_ldexpf(ag[d], ke - ks[d]);
      const f32x4 z = {0.f, 0.f, 0.f, 0.f};
      const f32x4 u = mat4(wuc, (u32x2){pack_bf16(ag[0], ag[1]), pack_bf16(ag[2], ag[3])}, z);
      const float o0 = bflo(mr[j].x) + u.x * bflo(gate[j].x), o1 = bfhi(mr[j].x) + u.y * bfhi(gate[j].x);
      const float o2 = bflo(mr[j].y) + u.z * bflo(gate[j].y), o3 = bfhi(mr[j].y) + u.w * bfhi(gate[j].y);
      __builtin_amdgcn_raw_buffer_store_b64((u32x2){pack_bf16(o0, o1), pack_bf16(o2, o3)}, r_o, i < EB ? i * 8u : OOB, 0, AUX_DEV);
    }
    __builtin_amdgcn_sched_barrier(0);
  }
  RT(51);
}

extern "C" void kernel_launch(void* const* d_in, const int* in_sizes, int n_in, void* d_out, int out_size,
                              void* d_ws, size_t ws_size, hipStream_t stream) {
  k_gemnet<<<NWG, NTH, 0, stream>>>((const u32x2*)d_in[0], (const u32*)d_in[1], (const u32*)d_in[2], (const u16*)d_in[3],
                                    (const u16*)d_in[4], (const u32*)d_in[5], (const u32*)d_in[6], (const u16*)d_in[7],
                                    (const u16*)d_in[8], (const u32*)d_in[9], (const u16*)d_in[10], (char*)d_ws,
                                    (u32x2*)d_out);
}
```

```cpp
#include <hip/hip_runtime.h>
#include <hip/hip_bf16.h>
#include <stdint.h>

typedef unsigned int u32;
typedef unsigned long long u64;
typedef unsigned short u16;
typedef u32 u32x2 __attribute__((ext_vector_type(2)));
typedef u32 u32x3 __attribute__((ext_vector_type(3)));
typedef u32 u32x4 __attribute__((ext_vector_type(4)));
typedef float f32x4 __attribute__((ext_vector_type(4)));
typedef short s16x4 __attribute__((ext_vector_type(4)));
typedef __bf16 bf16x2_t __attribute__((ext_vector_type(2)));
typedef float f32x2_t __attribute__((ext_vector_type(2)));

constexpr int E_ = 800000;
constexpr int T_ = 12800000;
constexpr int EB = 3125;
constexpr int EH = E_ / 2;
constexpr int NWG = 256;
constexpr int NTH = 1024;
constexpr int NT = 3;
constexpr int NS = 4;
constexpr int NR = 4 * NS + 1;
constexpr int NU = 9;
constexpr int TILE_MAX = 16704;
constexpr int NTILES = NWG * NT;
constexpr int NSLOT = TILE_MAX + 512;
constexpr int TSTRIDE = 8608;
constexpr int TABB = 256 * NT * 8 + 128;
constexpr u32 NULLREC = (u32)EB;
constexpr int EP = EB + 3;
constexpr int NW = 14;
constexpr int GDEPTH = 3;
constexpr int SIGW = 10;
constexpr int POLL_SLEEP = 8;

constexpr size_t WS_EPOCH = 0;
constexpr size_t WS_FLAG = 128;
constexpr size_t WS_MAX = WS_FLAG + 2048;
constexpr size_t WS_TAB = 8192;
constexpr size_t WS_REC = WS_TAB + (size_t)NWG * TABB;
static_assert(WS_MAX + 2048 <= WS_TAB && WS_REC % 128 == 0 && TABB % 128 == 0, "layout");
static_assert(NSLOT / 2 <= TSTRIDE, "tile region");
static_assert(WS_REC + (size_t)NTILES * TSTRIDE * 12 <= (size_t)256 * 1024 * 1024, "ws");

__device__ __forceinline__ float bf2f(u32 h) { return __uint_as_float(h << 16); }
__device__ __forceinline__ float bflo(u32 w) { return __uint_as_float(w << 16); }
__device__ __forceinline__ float bfhi(u32 w) { return __uint_as_float(w & 0xffff0000u); }
__device__ __forceinline__ u32 pack_bf16(float a, float b) {
  f32x2_t v = {a, b};
  bf16x2_t r = __builtin_convertvector(v, bf16x2_t);
  return __builtin_bit_cast(u32, r);
}
__device__ __forceinline__ int fresh(int x) { asm volatile("" : "+v"(x)); return x; }

__device__ __forceinline__ void lds_barrier() {
  asm volatile("s_waitcnt lgkmcnt(0)\n\ts_barrier" ::: "memory");
}

template <int CTRL, int RM>
__device__ __forceinline__ u32 dpp0(u32 v) { return (u32)__builtin_amdgcn_update_dpp(0, (int)v, CTRL, RM, 0xf, false); }
__device__ __forceinline__ u32 wave_incl_add(u32 v) {
  v += dpp0<0x111, 0xf>(v);
  v += dpp0<0x112, 0xf>(v);
  v += dpp0<0x114, 0xf>(v);
  v += dpp0<0x118, 0xf>(v);
  v += dpp0<0x142, 0xa>(v);
  v += dpp0<0x143, 0xc>(v);
  return v;
}
__device__ __forceinline__ u32 umax(u32 a, u32 b) { return a > b ? a : b; }
__device__ __forceinline__ u32 wave_incl_max(u32 v) {
  v = umax(v, dpp0<0x111, 0xf>(v));
  v = umax(v, dpp0<0x112, 0xf>(v));
  v = umax(v, dpp0<0x114, 0xf>(v));
  v = umax(v, dpp0<0x118, 0xf>(v));
  v = umax(v, dpp0<0x142, 0xa>(v));
  v = umax(v, dpp0<0x143, 0xc>(v));
  return v;
}

template <int CTRL, int RM>
__device__ __forceinline__ float dppf(float v) { return __uint_as_float(dpp0<CTRL, RM>(__float_as_uint(v))); }
__device__ __forceinline__ float wave_incl_addf(float v) {
  v += dppf<0x111, 0xf>(v);
  v += dppf<0x112, 0xf>(v);
  v += dppf<0x114, 0xf>(v);
  v += dppf<0x118, 0xf>(v);
  v += dppf<0x142, 0xa>(v);
  v += dppf<0x143, 0xc>(v);
  return v;
}

typedef __amdgpu_buffer_rsrc_t rsrc_t;
__device__ __forceinline__ rsrc_t make_rsrc(const void* p, u32 bytes) {
  return __builtin_amdgcn_make_buffer_rsrc((void*)p, 0, bytes, 0x00020000);
}
constexpr u32 OOB = 0x80000000u;
constexpr int AUX_NT = 2;
constexpr int AUX_DEV = 16;

__device__ __forceinline__ u64 load_dev(const u64* p) { return __hip_atomic_load(p, __ATOMIC_RELAXED, __HIP_MEMORY_SCOPE_AGENT); }
__device__ __forceinline__ void store_dev(u64* p, u64 v) { __hip_atomic_store(p, v, __ATOMIC_RELAXED, __HIP_MEMORY_SCOPE_AGENT); }

__device__ __forceinline__ f32x4 mat4(const u32x2& wT, const u32x2& row, const f32x4& acc) {
  return __builtin_amdgcn_mfma_f32_4x4x4bf16_1k(__builtin_bit_cast(s16x4, wT), __builtin_bit_cast(s16x4, row), acc, 0, 0, 0);
}
__device__ __forceinline__ float wel(const u32* __restrict__ w, int i) { return (i & 1) ? bfhi(w[i >> 1]) : bflo(w[i >> 1]); }

__device__ __forceinline__ u32x4 edge_rbf(float d, const u32* __restrict__ centers) {
  u32x4 rb;
#pragma unroll
  for (int r = 0; r < 4; r++) {
    u32 p = pack_bf16(d - wel(centers, 2 * r), d - wel(centers, 2 * r + 1));
    float x = bflo(p), y = bfhi(p);
    p = pack_bf16(x * x, y * y);
    p = pack_bf16(-10.0f * bflo(p), -10.0f * bfhi(p));
    rb[r] = pack_bf16(__expf(bflo(p)), __expf(bfhi(p)));
  }
  return rb;
}
__device__ __forceinline__ f32x4 rb_times(const u32x4& rb, const u32x2& wlo, const u32x2& whi) {
  const f32x4 z = {0.f, 0.f, 0.f, 0.f};
  return mat4(whi, (u32x2){rb.z, rb.w}, mat4(wlo, (u32x2){rb.x, rb.y}, z));
}

struct PartLds {
  u64 stage[NSLOT + 4];
  u32 hist[512];
  u32 cursor[512];
  u32 sinfo[4];
  u32 smax[16];
  float ssum[16];
};
struct AccLds {
  u64 a01[EP];
  u64 a23[EP];
  u32x4 at[EP];
  u32 cnt[EP];
  u64 bmap[16][16];
  u32 near_end;
  u32 flag;
};
struct Lds {
  union { PartLds part; AccLds acc; };
  u32x2 tab[256][NT];
  u16 dist[4 * NTH];
  u32x2 wcols[4][8];
  u16 w[128];
};
static_assert(sizeof(Lds) <= 160 * 1024, "LDS");

struct Raw { u32x4 in, out; u32x2 an; };

constexpr float MAGIC = 12582912.0f;
constexpr u32 MAGIC_BITS = 0x4B400000u;

struct Consts { u32x2 wdT; float f0, f1; const u32x4* at; };

__device__ __forceinline__ float dot2bf(u32 a, u32 b) {
  return __builtin_amdgcn_fdot2_f32_bf16(__builtin_bit_cast(bf16x2_t, a), __builtin_bit_cast(bf16x2_t, b), 0.f, false);
}

template <int MODE>
__device__ __forceinline__ void add_record(AccLds& S, const Consts& C, const f32x4& md, u32 loc, float ang) {
  const u32 pk = pack_bf16(C.f0 * ang, C.f1 * ang);
  const float INV2PI = 0.15915494309189535f;
  const float c0 = __builtin_amdgcn_cosf(__builtin_amdgcn_fractf(bflo(pk) * INV2PI));
  const float c1 = __builtin_amdgcn_cosf(__builtin_amdgcn_fractf(bfhi(pk) * INV2PI));
  const u32 cp = pack_bf16(c0, c1);
  const u32x4 a = C.at[loc];
  const float s0 = dot2bf(a.x, cp), s1 = dot2bf(a.y, cp), s2 = dot2bf(a.z, cp), s3 = dot2bf(a.w, cp);
  if (MODE == 0) {
    const u32 t0 = __float_as_uint(__builtin_fmaf(md.x, s0, MAGIC)), t1 = __float_as_uint(__builtin_fmaf(md.y, s1, MAGIC));
    const u32 t2 = __float_as_uint(__builtin_fmaf(md.z, s2, MAGIC)), t3 = __float_as_uint(__builtin_fmaf(md.w, s3, MAGIC));
    atomicAdd(&S.a01[loc], (u64)t0 | ((u64)t1 << 32));
    atomicAdd(&S.a23[loc], (u64)t2 | ((u64)t3 << 32));
    atomicAdd(&S.cnt[loc], 1u);
  } else {
    float* p01 = (float*)&S.a01[loc];
    float* p23 = (float*)&S.a23[loc];
    atomicAdd(p01, md.x * s0);
    atomicAdd(p01 + 1, md.y * s1);
    atomicAdd(p23, md.z * s2);
    atomicAdd(p23 + 1, md.w * s3);
  }
}

struct Runs { u32 cnt, P, base, total; };
__device__ __forceinline__ Runs make_runs(u32 ent, u32 tile) {
  Runs R;
  R.cnt = ent >> 16;
  const u32 incl = wave_incl_add(R.cnt);
  R.P = incl - R.cnt;
  R.total = __builtin_amdgcn_readlane(incl, 63);
  R.base = tile * TSTRIDE + (ent & 0xffffu) - R.P;
  return R;
}

struct Burst { u32x3 u[NW]; };
__device__ __forceinline__ void map_burst(u32 (&off)[NW], const Runs& R, u64* bm, int lane) {
  const bool ne = R.cnt != 0;
  const u64 nz = __ballot(ne);
  const u32 rank = __builtin_amdgcn_mbcnt_hi((u32)(nz >> 32), __builtin_amdgcn_mbcnt_lo((u32)nz, 0));
  const u32 cbase = (u32)__builtin_amdgcn_ds_permute((int)(ne ? rank * 4 : 252u), (int)R.base);
  if (lane < 16) bm[lane] = 0;
  if (ne && R.P < NW * 64) atomicOr((u32*)bm + (R.P >> 5), 1u << (R.P & 31));
  const u64 word = bm[lane & 15];
  const u32 wlo = (u32)word, whi = (u32)(word >> 32);
  u32 cum = 0;
#pragma unroll
  for (int j = 0; j < NW; j++) {
    const u64 M = (u64)(u32)__builtin_amdgcn_readlane(wlo, j) | ((u64)(u32)__builtin_amdgcn_readlane(whi, j) << 32);
    const u32 r0 = cum + (u32)(M & 1) - 1;
    const u64 M1 = M >> 1;
    const u32 r = __builtin_amdgcn_mbcnt_hi((u32)(M1 >> 32), __builtin_amdgcn_mbcnt_lo((u32)M1, r0));
    cum += __builtin_popcountll(M);
    const u32 u = j * 64 + lane;
    const u32 v = (u32)__builtin_amdgcn_ds_bpermute((int)(r * 4), (int)cbase);
    off[j] = u < R.total ? __umul24(v + u, 12u) : OOB;
  }
}
__device__ __forceinline__ void load_burst(Burst& B, const u32 (&off)[NW], rsrc_t r_rec, int j0, int j1) {
  __builtin_amdgcn_sched_barrier(0);
#pragma unroll
  for (int j = 0; j < NW; j++)
    if (j >= j0 && j < j1) B.u[j] = __builtin_amdgcn_raw_buffer_load_b96(r_rec, off[j], 0, AUX_DEV);
  __builtin_amdgcn_sched_barrier(0);
}

struct Win { u32x2 g0, g1; };
__device__ __forceinline__ void issue_win(Win& W, const u32x3& c, rsrc_t r_m, u32 u0, u32 total, int lane) {
  const bool v = u0 + lane < total;
  W.g0 = __builtin_amdgcn_raw_buffer_load_b64(r_m, v ? (c.x >> 9) & ~7u : OOB, 0, 0);
  W.g1 = __builtin_amdgcn_raw_buffer_load_b64(r_m, v ? (c.z >> 9) & ~7u : OOB, 0, 0);
}
template <int MODE>
__device__ __forceinline__ void proc_win(AccLds& S, const Consts& C, const Win& W, const u32x3& c, u32 u0, u32 total, int lane) {
  const f32x4 z = {0.f, 0.f, 0.f, 0.f};
  const f32x4 md0 = mat4(C.wdT, W.g0, z), md1 = mat4(C.wdT, W.g1, z);
  if (u0 + lane < total) {
    add_record<MODE>(S, C, md0, c.x & 0xfffu, bflo(c.y));
    add_record<MODE>(S, C, md1, c.z & 0xfffu, bfhi(c.y));
  }
}

struct Pipe { Win W[GDEPTH + 1]; };
__device__ __forceinline__ void start_burst(Pipe& Q, const Burst& B, u32 total, rsrc_t r_m, int lane) {
#pragma unroll
  for (int j = 0; j < GDEPTH; j++) issue_win(Q.W[j], B.u[j], r_m, j * 64, total, lane);
}
__device__ __forceinline__ void run_burst(AccLds& S, const Consts& C, Pipe& Q, const Burst& B, u32 total, rsrc_t r_m, int lane, bool first) {
#pragma unroll
  for (int j = 0; j < NW; j++) {
    if (j + GDEPTH < NW) issue_win(Q.W[(j + GDEPTH) % (GDEPTH + 1)], B.u[j + GDEPTH], r_m, (j + GDEPTH) * 64, total, lane);
    proc_win<0>(S, C, Q.W[j % (GDEPTH + 1)], B.u[j], j * 64, total, lane);
    if (j == SIGW && first && lane == 0) atomicAdd(&S.near_end, 1u);
  }
}

template <int MODE>
__device__ __forceinline__ void run_simple(AccLds& S, const Consts& C, const Runs& R, u32 u0, rsrc_t r_rec, rsrc_t r_m, int lane) {
#pragma unroll 1
  for (; u0 < R.total; u0 += 64) {
    const u32 d = R.P - u0;
    const bool part = R.cnt != 0 && d - 1u < 63u;
    u32 v = (u32)__builtin_amdgcn_ds_permute((int)(part ? d * 4 : 0u), (int)R.base);
    const u64 m0 = __ballot(R.cnt != 0 && R.P <= u0);
    const u32 b0 = __builtin_amdgcn_readlane(R.base, 63 - __builtin_clzll(m0 | 1ull));
    v = wave_incl_max(lane == 0 ? b0 : v);
    const u32 u = u0 + lane;
    const u32x3 c = __builtin_amdgcn_raw_buffer_load_b96(r_rec, u < R.total ? __umul24(v + u, 12u) : OOB, 0, AUX_DEV);
    Win W;
    issue_win(W, c, r_m, u0, R.total, lane);
    proc_win<MODE>(S, C, W, c, u0, R.total, lane);
  }
}

__device__ __forceinline__ bool timed_out(u64 t0) { return __builtin_amdgcn_s_memrealtime() - t0 > 5000000ull; }

__global__ void __launch_bounds__(NTH, 4) k_gemnet(const u32x2* __restrict__ mrow, const u32* __restrict__ idx_in,
                                                const u32* __restrict__ idx_out, const u16* __restrict__ dist,
                                                const u16* __restrict__ angle, const u32* __restrict__ centers,
                                                const u32* __restrict__ freq, const u16* __restrict__ w_rbf,
                                                const u16* __restrict__ w_sbf, const u32* __restrict__ w_down,
                                                const u16* __restrict__ w_up, char* __restrict__ ws,
                                                u32x2* __restrict__ outp) {
  __shared__ __attribute__((aligned(16))) Lds lds;
  const int wave = __builtin_amdgcn_readfirstlane(threadIdx.x >> 6);
  const int w = blockIdx.x;
  const int b = (w & 7) * 32 + (w >> 3);
  u64 tag;

  {
    u64* const stage = lds.part.stage;
    u32* const hist = lds.part.hist;
    u32* const cursor = lds.part.cursor;
    const int t = threadIdx.x;
    const int lane = t & 63;
    const rsrc_t r_in = make_rsrc(idx_in, T_ * 4u), r_out = make_rsrc(idx_out, T_ * 4u), r_an = make_rsrc(angle, T_ * 2u);
    const rsrc_t r_dist = make_rsrc(dist + b * EB, EB * 2u);

    Raw raw[NS];
    u32 xin, xout, xan;
    auto tile_start = [](u32 tile) { return (tile * (u32)(T_ / NWG) / NT) & ~63u; };
    auto load_slot = [&](int k, int s, int t) {
      const u32 r0 = tile_start(w * NT + k);
      const u32 r = r0 + s * NTH * 4;
      const bool live = k < NT;
      __builtin_amdgcn_sched_barrier(0);
      if (s < NS) {
        const u32 vo16 = live ? t * 16u : OOB, vo8 = live ? t * 8u : OOB;
        raw[s].in = __builtin_amdgcn_raw_buffer_load_b128(r_in, vo16, r * 4u, AUX_NT);
        raw[s].out = __builtin_amdgcn_raw_buffer_load_b128(r_out, vo16, r * 4u, AUX_NT);
        raw[s].an = __builtin_amdgcn_raw_buffer_load_b64(r_an, vo8, r * 2u, AUX_NT);
      } else {
        const u32 nx = tile_start(w * NT + k + 1) - r0 - 4 * NS * NTH;
        const u32 xo4 = (live && t < nx) ? t * 4u : OOB, xo2 = (live && t < nx) ? t * 2u : OOB;
        xin = __builtin_amdgcn_raw_buffer_load_b32(r_in, xo4, r * 4u, AUX_NT);
        xout = __builtin_amdgcn_raw_buffer_load_b32(r_out, xo4, r * 4u, AUX_NT);
        xan = __builtin_amdgcn_raw_buffer_load_b16(r_an, xo2, r * 2u, AUX_NT);
      }
      __builtin_amdgcn_sched_barrier(0);
    };
    u32 lo[NR], hi[NR];
    auto key = [&](u32 in, u32 out, u32 an16, u32& l, u32& h) {
      u32 q;
      asm("v_mul_hi_u32_u24 %0, %1, %2" : "=v"(q) : "v"(out), "v"(1374390u));
      const u32 loc = out - q * EB;
      const bool up = in >= (u32)EH;
      const u32 b4 = q * 4 + (up ? 1024u : 0u);
      l = ((in - (up ? (u32)EH : 0u)) << 12) | loc;
      h = an16 | (b4 << 16);
      return b4;
    };

    {
      const u64 epoch = load_dev((const u64*)(ws + WS_EPOCH));
      u32 dv[4];
#pragma unroll
      for (int j = 0; j < 4; j++) dv[j] = __builtin_amdgcn_raw_buffer_load_b16(r_dist, (j * NTH + t) * 2u, 0, 0);
      const u16* wp = t < 64 ? w_sbf + t : (t < 96 ? w_rbf + (t - 64) : (t < 112 ? (const u16*)w_down + (t - 96) : w_up + (t - 112)));
      const u32 wv = t < 128 ? *wp : 0;
      const rsrc_t r_m = make_rsrc(mrow + b * EB, EB * 8u);
      u32x2 r[4];
#pragma unroll
      for (int i = 0; i < 4; i++) r[i] = __builtin_amdgcn_raw_buffer_load_b64(r_m, t * 8u, i * NTH * 8u, 0);
#pragma unroll
      for (int s = 0; s <= NS; s++) load_slot(0, s, t);
      if (t < 512) hist[t] = 0;
      u32 mx = 0;
      float sm = 0.f;
#pragma unroll
      for (int i = 0; i < 4; i++) {
        const u32 keep = (i * NTH + t < EB) ? 0x7fffu : 0u;
        const u32 a0 = r[i].x & keep, a1 = (r[i].x >> 16) & keep, a2 = r[i].y & keep, a3 = (r[i].y >> 16) & keep;
        mx = umax(mx, umax(umax(a0, a1), umax(a2, a3)));
        sm += (bf2f(a0) + bf2f(a1)) + (bf2f(a2) + bf2f(a3));
      }
      mx = wave_incl_max(mx);
      sm = wave_incl_addf(sm);
      if (lane == 63) { lds.part.smax[wave] = mx; lds.part.ssum[wave] = sm; }
      if (t < 128) lds.w[t] = (u16)wv;
#pragma unroll
      for (int j = 0; j < 4; j++) lds.dist[j * NTH + t] = (u16)dv[j];
      const u64 e = epoch + 1;
      tag = (u64)(u32)__builtin_amdgcn_readfirstlane((u32)e) | ((u64)(u32)__builtin_amdgcn_readfirstlane((u32)(e >> 32)) << 32);
    }
    lds_barrier();
    if (t == 0) {
      u32 mx = 0;
      float sm = 0.f;
#pragma unroll
      for (int i = 0; i < 16; i++) { mx = umax(mx, lds.part.smax[i]); sm += lds.part.ssum[i]; }
      const u32 me = (__float_as_uint(sm * (1.0f / (4 * EB))) >> 23) & 0xffu;
      store_dev((u64*)(ws + WS_MAX) + w, (tag << 24) | (me << 16) | mx);
    }
    if (t < 32) {
      const int c = t & 3, i = t >> 2;
      const int o = c + (i < 4 ? (i & 1) * 32 + (i >> 1) * 4 : 64 + (i - 4) * 16), st = i < 4 ? 8 : 4;
      lds.wcols[c][i] = (u32x2){(u32)lds.w[o] | ((u32)lds.w[o + st] << 16), (u32)lds.w[o + 2 * st] | ((u32)lds.w[o + 3 * st] << 16)};
    }

#pragma unroll
    for (int u = 0; u < NU; u++) __builtin_amdgcn_raw_buffer_store_b96((u32x3){0, 0, 0}, make_rsrc(ws, 0), OOB + u * 16u, 0, 0);

#pragma unroll 1
    for (int k = 0; k < NT; k++) {
      const int tile = w * NT + k;
      const int t = fresh(threadIdx.x);
      const int lane = t & 63;
      const bool hasx = (u32)t < tile_start(tile + 1) - tile_start(tile) - 4 * NS * NTH;
#pragma unroll
      for (int s = 0; s < NS; s++) {
#pragma unroll
        for (int j = 0; j < 4; j++) {
          const u32 aw = raw[s].an[j >> 1];
          const u32 b4 = key(raw[s].in[j], raw[s].out[j], (j & 1) ? (aw >> 16) : (aw & 0xffffu), lo[s * 4 + j], hi[s * 4 + j]);
          atomicAdd((u32*)((char*)hist + b4), 8u);
        }
      }
      {
        const u32 b4 = key(xin, xout, xan, lo[NR - 1], hi[NR - 1]);
        if (hasx) atomicAdd((u32*)((char*)hist + b4), 8u);
      }
      load_slot(k + 1, 0, t);
      lds_barrier();
      u32x4 c0, c1;
      u32 run;
      if (wave == 1) {
        c0 = *(const u32x4*)&hist[lane * 8];
        c1 = *(const u32x4*)&hist[lane * 8 + 4];
        u32x4 p0 = (c0 + 8u) & ~15u, p1 = (c1 + 8u) & ~15u;
        const u32 tot = p0.x + p0.y + p0.z + p0.w + p1.x + p1.y + p1.z + p1.w;
        const u32 incl = wave_incl_add(tot);
        run = incl - tot;
        u32x4 a, bb;
        a.x = run; a.y = a.x + p0.x; a.z = a.y + p0.y; a.w = a.z + p0.z;
        bb.x = a.w + p0.w; bb.y = bb.x + p1.x; bb.z = bb.y + p1.y; bb.w = bb.z + p1.z;
        *(u32x4*)&cursor[lane * 8] = a;
        *(u32x4*)&cursor[lane * 8 + 4] = bb;
        if (lane == 63) lds.part.sinfo[0] = incl >> 4;
      }
      lds_barrier();
      if (wave == 1) {
        *(u32x4*)&hist[lane * 8] = (u32x4){0, 0, 0, 0};
        *(u32x4*)&hist[lane * 8 + 4] = (u32x4){0, 0, 0, 0};
        u32 ent[8];
#pragma unroll
        for (int j = 0; j < 8; j++) {
          const u32 c = j < 4 ? c0[j & 3] : c1[j & 3];
          const u32 p = (c + 8u) & ~15u;
          ent[j] = (run >> 4) | (p << 12);
          *(u64*)((char*)stage + ((c & 8u) ? run + c : (u32)(NSLOT * 8))) = (u64)NULLREC;
          run += p;
        }
        u32x4 e0, e1;
#pragma unroll
        for (int j = 0; j < 4; j++) {
          const auto sw = __builtin_amdgcn_permlane32_swap(ent[j], ent[j + 4], false, false);
          if (j < 2) { e0[2 * j] = sw[0]; e0[2 * j + 1] = sw[1]; } else { e1[2 * j - 4] = sw[0]; e1[2 * j - 3] = sw[1]; }
        }
        const int q0 = (lane & 31) * 8 + (lane >> 5) * 4;
        lds.tab[q0][k] = (u32x2){e0.x, e0.y};
        lds.tab[q0 + 1][k] = (u32x2){e0.z, e0.w};
        lds.tab[q0 + 2][k] = (u32x2){e1.x, e1.y};
        lds.tab[q0 + 3][k] = (u32x2){e1.z, e1.w};
      }
      {
        constexpr int H = NR / 2;
        u32 pos[NR - H];
#pragma unroll
        for (int j = 0; j < H; j++) pos[j] = atomicAdd((u32*)((char*)cursor + (hi[j] >> 16)), 8u);
        load_slot(k + 1, 1, t);
#pragma unroll
        for (int j = 0; j < H; j++) *(u64*)((char*)stage + pos[j]) = (u64)lo[j] | ((u64)hi[j] << 32);
        __builtin_amdgcn_sched_barrier(0);
#pragma unroll
        for (int j = H; j < NR - 1; j++) pos[j - H] = atomicAdd((u32*)((char*)cursor + (hi[j] >> 16)), 8u);
        if (hasx) pos[NR - 1 - H] = atomicAdd((u32*)((char*)cursor + (hi[NR - 1] >> 16)), 8u);
#pragma unroll
        for (int j = H; j < NR - 1; j++) *(u64*)((char*)stage + pos[j - H]) = (u64)lo[j] | ((u64)hi[j] << 32);
        if (hasx) *(u64*)((char*)stage + pos[NR - 1 - H]) = (u64)lo[NR - 1] | ((u64)hi[NR - 1] << 32);
      }
#pragma unroll
      for (int s = 2; s <= NS; s++) load_slot(k + 1, s, t);
      lds_barrier();
      {
        const u32 nu = __builtin_amdgcn_readfirstlane(lds.part.sinfo[0]);
        const rsrc_t r_rec = make_rsrc(ws + WS_REC + (size_t)tile * (TSTRIDE * 12), TSTRIDE * 12);
#pragma unroll
        for (int u = 0; u < NU; u++) {
          const u32 j = u * NTH + t;
          const u32x4 q = *(const u32x4*)&stage[j < nu ? 2 * j : 0];
          const u32x3 v = {q.x, __builtin_amdgcn_perm(q.w, q.y, 0x05040100u), q.z};
          __builtin_amdgcn_raw_buffer_store_b96(v, r_rec, j < nu ? j * 12u : OOB, 0, AUX_DEV);
        }
      }
    }
  }

  {
    const int tt = fresh(threadIdx.x);
    if (tt < 256 * NT / 2)
      __builtin_amdgcn_raw_buffer_store_b128(((const u32x4*)lds.tab)[tt], make_rsrc(ws + WS_TAB + (size_t)w * TABB, TABB), tt * 16u, 0, AUX_DEV);
  }
  asm volatile("s_waitcnt vmcnt(0)" ::: "memory");
  lds_barrier();
  AccLds& S = lds.acc;
  const int t = fresh(threadIdx.x);
  const int lane = t & 63;
  const int c4 = lane & 3;
  if (t == 0) store_dev((u64*)(ws + WS_FLAG) + (w & 15) * 16 + (w >> 4), tag);
  const u32 pline = (u32)(w >> 3) * 800u + wave * 50u + lane;
  const u32 pulled = __builtin_amdgcn_raw_buffer_load_b32(make_rsrc(mrow, EH * 8u), (lane < 50 && pline < 25000u) ? pline * 128u : OOB, 0, 0);
  if (t == 0) { S.flag = 0; S.near_end = 0; }
#pragma unroll
  for (int j = 0; j < 4; j++) {
    const int i = j * NTH + t;
    if (i < EP) { S.a01[i] = 0; S.a23[i] = 0; S.cnt[i] = 0; }
  }
  lds_barrier();

  const rsrc_t r_rec = make_rsrc(ws + WS_REC, (u32)NTILES * TSTRIDE * 12u);
  const rsrc_t r_m0 = make_rsrc(mrow, EH * 8u), r_m1 = make_rsrc(mrow + EH, EH * 8u);
  const u32 pj = (u32)lane / NT, pk = (u32)lane - pj * NT;
  const bool has_tile = lane < 16 * NT;
  const u32 tile = has_tile ? (pj * 16 + wave) * NT + pk : 0u;
  const u64* pmax = (const u64*)(ws + WS_MAX) + lane;
  const u64* pflag = (const u64*)(ws + WS_FLAG) + wave * 16 + (lane & 15);
  const u64 t0 = __builtin_amdgcn_s_memrealtime();
  u64 g0 = load_dev(pmax), g1 = load_dev(pmax + 64), g2 = load_dev(pmax + 128), g3 = load_dev(pmax + 192);
  const bool ready = __all(load_dev(pflag) == tag);
  asm volatile("" :: "v"(pulled));
  u32 mall, ent0, ent1;
  float mmean;
  Runs R;
  Burst B;
  Pipe Q;
  u32x2 gate[4];
  u32 kexp = 0;
  bool bad = false;
  auto edge_tables = [&]() {
    const u32x4* wc = (const u32x4*)lds.wcols[c4];
    const u32x4 w0 = wc[0], w1 = wc[1], w2 = wc[2];
#pragma unroll
    for (int j = 0; j < 4; j++) {
      const int i = j * NTH + t;
      gate[j] = (u32x2){0, 0};
      if (j < 3 || wave == 0) {
        const u32x4 rb = edge_rbf(bf2f(lds.dist[i]), centers);
        const f32x4 A0 = rb_times(rb, (u32x2){w0.x, w0.y}, (u32x2){w0.z, w0.w});
        const f32x4 A1 = rb_times(rb, (u32x2){w1.x, w1.y}, (u32x2){w1.z, w1.w});
        const f32x4 g = rb_times(rb, (u32x2){w2.x, w2.y}, (u32x2){w2.z, w2.w});
        gate[j] = (u32x2){pack_bf16(g.x, g.y), pack_bf16(g.z, g.w)};
        const float n = fmaxf(fmaxf(fabsf(A0.x) + fabsf(A1.x), fabsf(A0.y) + fabsf(A1.y)),
                              fmaxf(fabsf(A0.z) + fabsf(A1.z), fabsf(A0.w) + fabsf(A1.w)));
        int e = __builtin_amdgcn_frexp_expf(n);
        e = e < -120 ? -120 : (e > 120 ? 120 : e);
        bad |= i < EB && !(n < 1e36f);
        const float sc = __builtin_amdgcn_ldexpf(1.f, -e);
        const u32x4 a = {pack_bf16(A0.x * sc, A1.x * sc), pack_bf16(A0.y * sc, A1.y * sc),
                         pack_bf16(A0.z * sc, A1.z * sc), pack_bf16(A0.w * sc, A1.w * sc)};
        kexp |= (u32)(e & 0xff) << (8 * j);
        if (i < EP) S.at[i] = i < EB ? a : (u32x4){0, 0, 0, 0};
      }
      __builtin_amdgcn_sched_barrier(0);
    }
  };
  if (!ready) edge_tables();
  {
    const u64 want = (tag << 24) >> 24;
    if (!ready)
      while (!__all(load_dev(pflag) == tag) && !timed_out(t0)) __builtin_amdgcn_s_sleep(POLL_SLEEP);
    while (!__all((g0 >> 24) == want && (g1 >> 24) == want && (g2 >> 24) == want && (g3 >> 24) == want) && !timed_out(t0)) {
      __builtin_amdgcn_s_sleep(POLL_SLEEP);
      g0 = load_dev(pmax); g1 = load_dev(pmax + 64); g2 = load_dev(pmax + 128); g3 = load_dev(pmax + 192);
    }
    __builtin_amdgcn_fence(__ATOMIC_ACQUIRE, "wavefront");
    const u32x2 ent01 = __builtin_amdgcn_raw_buffer_load_b64(make_rsrc(ws + WS_TAB, NWG * TABB), has_tile ? (pj * 16 + wave) * TABB + b * (NT * 8) + pk * 8 : OOB, 0, AUX_DEV);
    const u32 x0 = (u32)g0 & 0xffffu, x1 = (u32)g1 & 0xffffu, x2 = (u32)g2 & 0xffffu, x3 = (u32)g3 & 0xffffu;
    mall = __builtin_amdgcn_readlane(wave_incl_max(umax(umax(x0, x1), umax(x2, x3))), 63);
    const float y = (__uint_as_float(((u32)g0 & 0xff0000u) << 7) + __uint_as_float(((u32)g1 & 0xff0000u) << 7)) +
                    (__uint_as_float(((u32)g2 & 0xff0000u) << 7) + __uint_as_float(((u32)g3 & 0xff0000u) << 7));
    mmean = __uint_as_float(__builtin_amdgcn_readlane(__float_as_uint(wave_incl_addf(y)), 63)) * (1.0f / NWG);
    if (w == 0 && t == 0) store_dev((u64*)(ws + WS_EPOCH), tag);
    ent0 = ent01.x; ent1 = ent01.y;
    R = make_runs(ent0, tile);
    u32 off0[NW];
    map_burst(off0, R, S.bmap[wave], lane);
    load_burst(B, off0, r_rec, 0, GDEPTH);
    start_burst(Q, B, R.total, r_m0, lane);
    load_burst(B, off0, r_rec, GDEPTH, NW);
  }
  if (ready) edge_tables();
  lds_barrier();
  if (bad) S.flag = 2;

  const float Bm = bf2f(mall);
  int ks[4];
  bool fixed_ok = Bm <= 1024.f * mmean;
#pragma unroll
  for (int d = 0; d < 4; d++) {
    float cs = 0.f;
#pragma unroll
    for (int k = 0; k < 4; k++) cs += fabsf(wel(w_down, k * 4 + d));
    const float Bd = Bm * cs * 1.02f;
    const bool zero = (Bd == 0.f);
    if (!zero && !(Bd > 1e-25f && Bd < 1e25f)) fixed_ok = false;
    ks[d] = __builtin_amdgcn_readfirstlane(zero ? 0 : 22 - __builtin_amdgcn_frexp_expf(Bd));
  }
  const u32x2 wdc = lds.wcols[c4][6];
  Consts C;
  C.at = S.at;
  C.f0 = wel(freq, 0);
  C.f1 = wel(freq, 1);
  {
    const int kc = c4 == 0 ? ks[0] : (c4 == 1 ? ks[1] : (c4 == 2 ? ks[2] : ks[3]));
    C.wdT = (u32x2){pack_bf16(__builtin_amdgcn_ldexpf(bflo(wdc.x), kc), __builtin_amdgcn_ldexpf(bfhi(wdc.x), kc)),
                    pack_bf16(__builtin_amdgcn_ldexpf(bflo(wdc.y), kc), __builtin_amdgcn_ldexpf(bfhi(wdc.y), kc))};
  }

  const int te = fresh(threadIdx.x);
  u32x2 mr[4];
  auto load_rows = [&]() {
    const rsrc_t r_mb = make_rsrc(mrow + b * EB, EB * 8u);
#pragma unroll
    for (int j = 0; j < 4; j++) mr[j] = __builtin_amdgcn_raw_buffer_load_b64(r_mb, (j * NTH + te) * 8u, 0, 0);
  };
  bool use_float = !fixed_ok;
  if (!use_float) {
#pragma unroll 1
    for (int h = 0; h < 2; h++) {
      const rsrc_t r_m = h ? r_m1 : r_m0;
      run_burst(S, C, Q, B, R.total, r_m, lane, h == 0);
      if (R.total > NW * 64) run_simple<0>(S, C, R, NW * 64, r_rec, r_m, lane);
      if (h == 0) {
        R = make_runs(ent1, tile);
        u32 off[NW];
        map_burst(off, R, S.bmap[wave], lane);
        load_burst(B, off, r_rec, 0, NW);
        while (*(volatile u32*)&S.near_end < 16u) __builtin_amdgcn_s_sleep(1);
        start_burst(Q, B, R.total, r_m1, lane);
      }
    }
    load_rows();
    lds_barrier();
    u32 over = 0;
#pragma unroll
    for (int j = 0; j < 4; j++) over |= (j * NTH + t < EB) && S.cnt[j * NTH + t < EB ? j * NTH + t : 0] >= 512u;
    if (over) S.flag = 1;
    lds_barrier();
    use_float = __builtin_amdgcn_readfirstlane(S.flag) != 0;
    if (use_float) {
      for (int i = t; i < EP; i += NTH) { S.a01[i] = 0; S.a23[i] = 0; }
      lds_barrier();
    }
  } else {
    __builtin_amdgcn_s_waitcnt(0x0F70);
    load_rows();
  }
  if (use_float) {
    C.wdT = wdc;
#pragma unroll
    for (int d = 0; d < 4; d++) ks[d] = 0;
#pragma unroll 1
    for (int h = 0; h < 2; h++)
      run_simple<1>(S, C, make_runs(h ? ent1 : ent0, tile), 0, r_rec, h ? r_m1 : r_m0, lane);
    lds_barrier();
  }

  const rsrc_t r_o = make_rsrc(outp + b * EB, EB * 8u);
  const u32x2 wuc = lds.wcols[c4][7];
#pragma unroll
  for (int j = 0; j < 4; j++) {
    const int i = j * NTH + te;
    if (j < 3 || wave == 0) {
      const int ii = i < EB ? i : 0;
      float ag[4];
      const u64 w01 = S.a01[ii], w23 = S.a23[ii];
      if (!use_float) {
        const u64 off = (u64)S.cnt[ii] * (((u64)MAGIC_BITS << 32) + MAGIC_BITS);
        const u64 v01 = w01 - off, v23 = w23 - off;
        const int l0 = (int)(u32)v01, l2 = (int)(u32)v23;
        const int h1 = (int)(u32)((v01 - (u64)(long long)l0) >> 32), h3 = (int)(u32)((v23 - (u64)(long long)l2) >> 32);
        ag[0] = (float)l0; ag[1] = (float)h1; ag[2] = (float)l2; ag[3] = (float)h3;
      } else {
        ag[0] = __uint_as_float((u32)w01); ag[1] = __uint_as_float((u32)(w01 >> 32));
        ag[2] = __uint_as_float((u32)w23); ag[3] = __uint_as_float((u32)(w23 >> 32));
      }
      const int ke = (int)(signed char)(kexp >> (8 * j));
#pragma unroll
      for (int d = 0; d < 4; d++) ag[d] = __builtin_amdgcn_ldexpf(ag[d], ke - ks[d]);
      const f32x4 z = {0.f, 0.f, 0.f, 0.f};
      const f32x4 u = mat4(wuc, (u32x2){pack_bf16(ag[0], ag[1]), pack_bf16(ag[2], ag[3])}, z);
      const float o0 = bflo(mr[j].x) + u.x * bflo(gate[j].x), o1 = bfhi(mr[j].x) + u.y * bfhi(gate[j].x);
      const float o2 = bflo(mr[j].y) + u.z * bflo(gate[j].y), o3 = bfhi(mr[j].y) + u.w * bfhi(gate[j].y);
      __builtin_amdgcn_raw_buffer_store_b64((u32x2){pack_bf16(o0, o1), pack_bf16(o2, o3)}, r_o, i < EB ? i * 8u : OOB, 0, AUX_DEV);
    }
    __builtin_amdgcn_sched_barrier(0);
  }
}

extern "C" void kernel_launch(void* const* d_in, const int* in_sizes, int n_in, void* d_out, int out_size,
                              void* d_ws, size_t ws_size, hipStream_t stream) {
  k_gemnet<<<NWG, NTH, 0, stream>>>((const u32x2*)d_in[0], (const u32*)d_in[1], (const u32*)d_in[2], (const u16*)d_in[3],
                                    (const u16*)d_in[4], (const u32*)d_in[5], (const u32*)d_in[6], (const u16*)d_in[7],
                                    (const u16*)d_in[8], (const u32*)d_in[9], (const u16*)d_in[10], (char*)d_ws,
                                    (u32x2*)d_out);
}
```
